```python
import torch

def _softmax(x, axis=-1):
    return torch.softmax(x, dim=axis)
def _gelu(x):
    return torch.nn.functional.gelu(x, approximate="tanh")
def _silu(x):
    return torch.nn.functional.silu(x)
def _sigmoid(x):
    return torch.sigmoid(x)
def _layernorm(x, axis=-1, eps=1e-5):
    m = x.mean(dim=axis, keepdim=True)
    v = x.var(dim=axis, keepdim=True, unbiased=False)
    return (x - m) / torch.sqrt(v + eps)
def _scatter_add(tgt, idx, src, axis=0):
    return tgt.clone().index_add_(axis, idx, src)
def _segment_sum(src, seg, n):
    out = torch.zeros((n,) + src.shape[1:], dtype=src.dtype)
    return out.index_add_(0, seg, src)
def _pad(x, pw):
    flat = tuple(v for pair in reversed(pw) for v in pair)
    return torch.nn.functional.pad(x, flat)

kernel_name = 'standalone__cross_network_dcn_v2__recsys'

def setup_inputs():

    _w = (lambda a: (lambda t: t.to(torch.bfloat16) if t.is_floating_point() else t)(torch.as_tensor(a)))
    B, F, D, L = 16384, 100, 32, 6
    Din = min(F * D, 512)
    R = max(Din // 4, 2)
    sc = Din ** -0.5
    return {
        "x0": _w((lambda *s: torch.randn(*s, dtype=torch.bfloat16) * 0.05)(B, Din)),
        "U":  _w((lambda *s: torch.randn(*s, dtype=torch.bfloat16) * 1.6)(L, Din, R) * sc),
        "V":  _w((lambda *s: torch.randn(*s, dtype=torch.bfloat16) * 1.6)(L, R, Din) * sc),
        "bs": _w((lambda *s: torch.randn(*s, dtype=torch.bfloat16) * 0.05)(L, Din)),
    }

def reference(x0, U, V, bs):
    x = x0
    for l in range(U.shape[0]):
        h = torch.einsum('rd,bd->br', V[l], x)
        h = torch.einsum('dr,br->bd', U[l], h)
        x = x0 * (h + bs[l]) + x
    return x
```

```cpp
#include <hip/hip_runtime.h>
#include <hip/hip_bf16.h>

typedef __bf16   bf16x8 __attribute__((ext_vector_type(8)));
typedef __bf16   bf16x2 __attribute__((ext_vector_type(2)));
typedef float    f32x16 __attribute__((ext_vector_type(16)));
typedef float    f32x2  __attribute__((ext_vector_type(2)));
typedef unsigned u32x4  __attribute__((ext_vector_type(4)));
#define DI __device__ __forceinline__

constexpr int NB = 16384, DIN = 512, RK = 128, NL = 6;
constexpr int FR_PER_WAVE = 4 + NL * 64;
constexpr int XROW = 1040;
constexpr int HBASE = 64 * XROW;
constexpr int LDS_BYTES = HBASE + 16384;

DI int perm23(int m) { return (m & 0x13) | ((m & 4) << 1) | ((m & 8) >> 1); }

__global__ __launch_bounds__(256) void dcn_relayout(const unsigned short* __restrict__ U, const unsigned short* __restrict__ V,
                                                    const unsigned short* __restrict__ bs, u32x4* __restrict__ ws) {
  const int lane = threadIdx.x & 63;
  const int gw = blockIdx.x * 4 + (threadIdx.x >> 6);
  if (gw >= 4 * FR_PER_WAVE) return;
  const int w = gw / FR_PER_WAVE, idx = gw % FR_PER_WAVE;
  const int m = perm23(lane & 31), h = lane >> 5;
  u32x4 v;
  if (idx < 4) {
    const int d = 32 * (4 * idx + w) + m;
    unsigned e[6];
    for (int j = 0; j < 6; ++j) e[j] = h ? 0u : (unsigned)bs[j * DIN + d];
    v[0] = e[0] | (e[1] << 16); v[1] = e[2] | (e[3] << 16); v[2] = e[4] | (e[5] << 16); v[3] = 0;
  } else {
    const int f = idx - 4, l = f >> 6, g = f & 63;
    if (g < 32) {
      v = *(const u32x4*)(V + (size_t)l * 65536 + (32 * w + m) * 512 + 16 * g + 8 * h);
    } else {
      const int u = g - 32, t = u >> 3, kk = u & 7;
      const int ks2 = 2 * ((w + (kk >> 1)) & 3) + (kk & 1);
      v = *(const u32x4*)(U + (size_t)l * 65536 + (32 * (4 * t + w) + m) * 128 + 16 * ks2 + 8 * h);
    }
  }
  ws[(size_t)gw * 64 + lane] = v;
}

DI unsigned cvtpk(float lo, float hi) { f32x2 v = {lo, hi}; bf16x2 b = __builtin_convertvector(v, bf16x2); return __builtin_bit_cast(unsigned, b); }
#define MFMA(a, b, c) __builtin_amdgcn_mfma_f32_32x32x16_bf16((a), (b), (c), 0, 0, 0)

__global__ __launch_bounds__(256, 1) void dcn_main(const char* __restrict__ x0, const u32x4* __restrict__ ws, char* __restrict__ out) {
  __shared__ __attribute__((aligned(16))) char smem[LDS_BYTES];
  const int lane = threadIdx.x & 63, w = __builtin_amdgcn_readfirstlane(threadIdx.x >> 6);
  const int c = lane & 31, h = lane >> 5;
  const char* xg = x0 + (size_t)blockIdx.x * 65536;
  for (int r = 0; r < 16; ++r) {
    const int row = 16 * w + r;
    *(u32x4*)(smem + row * XROW + lane * 16) = *(const u32x4*)(xg + row * 1024 + lane * 16);
  }
  const u32x4* wp = ws + (size_t)w * FR_PER_WAVE * 64 + lane;
  bf16x8 bias[4];
  for (int t = 0; t < 4; ++t) bias[t] = __builtin_bit_cast(bf16x8, wp[t * 64]);
  wp += 4 * 64;
  __syncthreads();
  u32x4 x0p[4][2][2];
  f32x16 S[4][2];
  for (int t = 0; t < 4; ++t) for (int bt = 0; bt < 2; ++bt) {
    for (int s = 0; s < 2; ++s) x0p[t][bt][s] = *(const u32x4*)(smem + (32 * bt + c) * XROW + t * 256 + w * 64 + s * 32 + h * 16);
    for (int i = 0; i < 16; ++i) S[t][bt][i] = 1.0f;
  }
  for (int l = 0; l < NL; ++l) {
    f32x16 H[2];
    for (int bt = 0; bt < 2; ++bt) for (int i = 0; i < 16; ++i) H[bt][i] = 0.f;
    #pragma unroll
    for (int ks = 0; ks < 32; ++ks) {
      bf16x8 a = __builtin_bit_cast(bf16x8, wp[ks * 64]);
      for (int bt = 0; bt < 2; ++bt) {
        bf16x8 b = *(const bf16x8*)(smem + (32 * bt + c) * XROW + ks * 32 + h * 16);
        H[bt] = MFMA(a, b, H[bt]);
      }
    }
    for (int bt = 0; bt < 2; ++bt) for (int s = 0; s < 2; ++s) {
      u32x4 p;
      for (int j = 0; j < 4; ++j) p[j] = cvtpk(H[bt][8 * s + 2 * j], H[bt][8 * s + 2 * j + 1]);
      *(u32x4*)(smem + HBASE + w * 4096 + (s * 2 + bt) * 1024 + lane * 16) = p;
    }
    __syncthreads();
    u32x4 selv = {0, 0, 0, 0};
    selv[0] = l == 0 ? 0x3F80u : l == 1 ? 0x3F800000u : 0u;
    selv[1] = l == 2 ? 0x3F80u : l == 3 ? 0x3F800000u : 0u;
    selv[2] = l == 4 ? 0x3F80u : l == 5 ? 0x3F800000u : 0u;
    bf16x8 sel = __builtin_bit_cast(bf16x8, selv);
    bf16x8 hf[8][2];
    #pragma unroll
    for (int kk = 0; kk < 8; ++kk) for (int bt = 0; bt < 2; ++bt)
      hf[kk][bt] = *(const bf16x8*)(smem + HBASE + ((w + (kk >> 1)) & 3) * 4096 + ((kk & 1) * 2 + bt) * 1024 + lane * 16);
    #pragma unroll
    for (int t = 0; t < 4; ++t) {
      for (int bt = 0; bt < 2; ++bt) S[t][bt] = MFMA(bias[t], sel, S[t][bt]);
      #pragma unroll
      for (int kk = 0; kk < 8; ++kk) {
        bf16x8 a = __builtin_bit_cast(bf16x8, wp[(32 + t * 8 + kk) * 64]);
        for (int bt = 0; bt < 2; ++bt) S[t][bt] = MFMA(a, hf[kk][bt], S[t][bt]);
      }
      for (int bt = 0; bt < 2; ++bt) for (int s = 0; s < 2; ++s) {
        u32x4 p;
        for (int j = 0; j < 4; ++j) {
          const unsigned q = x0p[t][bt][s][j];
          const float lo = __builtin_bit_cast(float, q << 16), hi = __builtin_bit_cast(float, q & 0xffff0000u);
          p[j] = cvtpk(lo * S[t][bt][8 * s + 2 * j], hi * S[t][bt][8 * s + 2 * j + 1]);
        }
        *(u32x4*)(smem + (32 * bt + c) * XROW + t * 256 + w * 64 + s * 32 + h * 16) = p;
      }
    }
    wp += 64 * 64;
    __syncthreads();
  }
  char* og = out + (size_t)blockIdx.x * 65536;
  for (int r = 0; r < 16; ++r) {
    const int row = 16 * w + r;
    *(u32x4*)(og + row * 1024 + lane * 16) = *(const u32x4*)(smem + row * XROW + lane * 16);
  }
}

extern "C" void kernel_launch(void* const* d_in, const int* in_sizes, int n_in, void* d_out, int out_size, void* d_ws, size_t ws_size,
                              hipStream_t stream) {
  const unsigned short* U = (const unsigned short*)d_in[1];
  const unsigned short* V = (const unsigned short*)d_in[2];
  const unsigned short* bs = (const unsigned short*)d_in[3];
  dcn_relayout<<<FR_PER_WAVE, 256, 0, stream>>>(U, V, bs, (u32x4*)d_ws);
  dcn_main<<<NB / 64, 256, 0, stream>>>((const char*)d_in[0], (const u32x4*)d_ws, (char*)d_out);
}
```

```cpp
#include <hip/hip_runtime.h>
#include <hip/hip_bf16.h>

constexpr int NB = 16384;
constexpr int LDS_BYTES = 163840;

__global__ __launch_bounds__(256, 1) void dcn_main(const char* __restrict__ x0, const char* __restrict__ U, const char* __restrict__ V,
                                                   const char* __restrict__ bs, char* __restrict__ ws, char* __restrict__ out) {
  __shared__ __attribute__((aligned(16))) char smem[LDS_BYTES];
  const unsigned w = __builtin_amdgcn_readfirstlane(threadIdx.x >> 6);
  const unsigned blk = blockIdx.x;
  const unsigned ldsb = (unsigned)(size_t)smem;
  const unsigned long long ka = (unsigned long long)__builtin_amdgcn_kernarg_segment_ptr();
  asm volatile(
    "s_load_dwordx8 s[24:31], %0, 0x0\n\t"
    "s_load_dwordx4 s[12:15], %0, 0x20\n\t"
    "v_mbcnt_lo_u32_b32 v232, -1, 0\n\t"
    "v_mbcnt_hi_u32_b32 v232, -1, v232\n\t"
    "v_lshlrev_b32 v233, 4, v232\n\t"
    "s_getpc_b64 s[96:97]\n\t"
    "Lpc%=:\n\t"
    "s_and_b32 s96, s96, 0xffffff80\n\t"
    "s_and_b32 s97, s97, 0xffff\n\t"
    "s_mov_b32 s98, Lend%=-Lpc%=\n\t"
    "s_mov_b32 s99, 0x20000\n\t"
    "s_bfe_u32 s51, %3, 0x30003\n\t"
    "s_lshl_b32 s51, s51, 2\n\t"
    "s_add_u32 s51, s51, %1\n\t"
    "s_lshl_b32 s51, s51, 10\n\t"
    "buffer_load_dwordx4 a[248:251], v233, s[96:99], s51 offen\n\t"
    "v_and_b32 v236, 0x70, v233\n\t"
    "v_bfe_u32 v235, v232, 3, 2\n\t"
    "v_lshl_add_u32 v255, v235, 8, v236\n\t"
    "v_lshrrev_b32 v234, 5, v232\n\t"
    "v_lshl_add_u32 v248, v234, 14, v255\n\t"
    "v_lshl_add_u32 v237, v234, 10, v255\n\t"
    "v_lshl_add_u32 v255, v234, 13, v255\n\t"
    "s_mul_i32 s58, %1, 24320\n\t"
    "s_add_u32 s58, s58, %2\n\t"
    "s_add_u32 s58, s58, 66560\n\t"
    "s_mul_i32 s92, %1, 8320\n\t"
    "s_add_u32 s92, s92, %2\n\t"
    "s_lshl_b32 s93, %1, 14\n\t"
    "s_lshl_b32 s59, %1, 15\n\t"
    "s_mov_b32 s42, 65536\n\t"
    "s_mov_b32 s43, 0x20000\n\t"
    "s_mov_b32 s82, 131072\n\t"
    "s_mov_b32 s83, 0x20000\n\t"
    "s_mov_b32 s86, 131072\n\t"
    "s_mov_b32 s87, 0x20000\n\t"
    "s_and_b32 s16, %3, 1\n\t"
    "s_cmp_lt_u32 %3, 160\n\t"
    "s_cselect_b32 s17, 786432, 0\n\t"
    "s_bfe_u32 s21, %3, 0x20003\n\t"
    "s_bfe_u32 s22, %3, 0x20001\n\t"
    "s_sub_u32 s23, %1, s21\n\t"
    "s_and_b32 s23, s23, 3\n\t"
    "s_mul_i32 s19, s22, 6\n\t"
    "s_add_u32 s19, s19, s23\n\t"
    "s_add_u32 s19, s19, 5\n\t"
    "s_add_u32 s20, s19, 3\n\t"
    "s_lshl_b32 s51, s22, 1\n\t"
    "s_add_u32 s52, s51, 1\n\t"
    "s_cmp_eq_u32 s23, 0\n\t"
    "s_cselect_b32 s19, s51, s19\n\t"
    "s_cselect_b32 s20, s52, s20\n\t"
    "s_add_u32 s51, s23, 24\n\t"
    "s_add_u32 s52, s23, 28\n\t"
    "s_cmp_eq_u32 s22, 3\n\t"
    "s_cselect_b32 s19, s51, s19\n\t"
    "s_cselect_b32 s20, s52, s20\n\t"
    "s_add_u32 s19, s19, 32\n\t"
    "s_add_u32 s20, s20, 32\n\t"
    "s_lshr_b32 s51, s22, 1\n\t"
    "s_and_b32 s52, s22, 1\n\t"
    "s_lshl_b32 s53, s51, 4\n\t"
    "s_add_u32 s53, s53, s52\n\t"
    "s_lshl_b32 s23, %1, 2\n\t"
    "s_add_u32 s53, s53, s23\n\t"
    "s_add_u32 s23, s53, 2\n\t"
    "s_cmp_lg_u32 s16, 0\n\t"
    "s_cselect_b32 s19, s19, s53\n\t"
    "s_cselect_b32 s20, s20, s23\n\t"
    "s_lshl_b32 s51, s51, 7\n\t"
    "s_lshl_b32 s52, s52, 12\n\t"
    "s_add_u32 s51, s51, s52\n\t"
    "s_lshl_b32 s52, s21, 15\n\t"
    "s_add_u32 s51, s51, s52\n\t"
    "s_lshl_b32 s52, %1, 13\n\t"
    "s_add_u32 s51, s51, s52\n\t"
    "s_lshl_b32 s52, s22, 13\n\t"
    "s_add_u32 s52, s52, s59\n\t"
    "s_lshl_b32 s53, s21, 11\n\t"
    "s_add_u32 s52, s52, s53\n\t"
    "s_cmp_lg_u32 s16, 0\n\t"
    "s_cselect_b32 s18, s52, s51\n\t"
    "s_lshr_b32 s51, %3, 5\n\t"
    "s_lshl_b32 s52, s51, 17\n\t"
    "s_add_u32 s18, s18, s52\n\t"
    "s_add_u32 s18, s18, 131072\n\t"
    "s_mul_i32 s52, s21, 5\n\t"
    "s_add_u32 s52, s52, s51\n\t"
    "s_lshl_b32 s52, s52, 6\n\t"
    "s_add_u32 s19, s19, s52\n\t"
    "s_add_u32 s20, s20, s52\n\t"
    "s_lshl_b32 s19, s19, 10\n\t"
    "s_lshl_b32 s20, s20, 10\n\t"
    "s_lshl_b32 s51, %3, 16\n\t"
    "s_waitcnt lgkmcnt(0)\n\t"
    "s_add_u32 s40, s24, s51\n\t"
    "s_addc_u32 s41, s25, 0\n\t"
    "s_and_b32 s41, s41, 0xffff\n\t"
    "s_add_u32 s14, s14, s51\n\t"
    "s_addc_u32 s15, s15, 0\n\t"
    "s_mov_b32 s80, s28\n\t"
    "s_and_b32 s81, s29, 0xffff\n\t"
    "s_mov_b32 s84, s26\n\t"
    "s_and_b32 s85, s27, 0xffff\n\t"
    "s_lshr_b32 s51, %3, 3\n\t"
    "s_lshl_b32 s51, s51, 12\n\t"
    "s_lshl_b32 s52, %1, 10\n\t"
    "s_add_u32 s51, s51, s52\n\t"
    "buffer_load_dwordx4 a[240:243], v233, s[80:83], s51 offen\n\t"
    "buffer_load_dwordx4 a[244:247], v233, s[84:87], s51 offen\n\t"
    "s_add_u32 m0, s92, 0\n\t"
    "s_add_u32 s51, s93, 0\n\t"
    "buffer_load_dwordx4 v255, s[40:43], s51 offen nt lds\n\t"
    "s_add_u32 m0, s92, 1040\n\t"
    "s_add_u32 s51, s93, 1024\n\t"
    "buffer_load_dwordx4 v255, s[40:43], s51 offen nt lds\n\t"
    "s_add_u32 m0, s92, 2080\n\t"
    "s_add_u32 s51, s93, 2048\n\t"
    "buffer_load_dwordx4 v255, s[40:43], s51 offen nt lds\n\t"
    "s_add_u32 m0, s92, 3120\n\t"
    "s_add_u32 s51, s93, 3072\n\t"
    "buffer_load_dwordx4 v255, s[40:43], s51 offen nt lds\n\t"
    "s_add_u32 m0, s92, 4160\n\t"
    "s_add_u32 s51, s93, 4096\n\t"
    "buffer_load_dwordx4 v255, s[40:43], s51 offen nt lds\n\t"
    "s_add_u32 m0, s92, 5200\n\t"
    "s_add_u32 s51, s93, 5120\n\t"
    "buffer_load_dwordx4 v255, s[40:43], s51 offen nt lds\n\t"
    "s_add_u32 m0, s92, 6240\n\t"
    "s_add_u32 s51, s93, 6144\n\t"
    "buffer_load_dwordx4 v255, s[40:43], s51 offen nt lds\n\t"
    "s_add_u32 m0, s92, 7280\n\t"
    "s_add_u32 s51, s93, 7168\n\t"
    "buffer_load_dwordx4 v255, s[40:43], s51 offen nt lds\n\t"
    "s_cmp_lg_u32 s16, 0\n\t"
    "s_cselect_b32 s60, s26, s28\n\t"
    "s_cselect_b32 s61, s27, s29\n\t"
    "s_cselect_b64 vcc, -1, 0\n\t"
    "s_lshr_b32 s52, 2048, s16\n\t"
    "s_and_b32 s61, s61, 0xffff\n\t"
    "s_mov_b32 s62, s17\n\t"
    "s_mov_b32 s63, 0x20000\n\t"
    "v_cndmask_b32 v237, v237, v233, vcc\n\t"
    "s_add_u32 m0, s58, 0\n\t"
    "s_add_u32 s51, s18, 0\n\t"
    "buffer_load_dwordx4 v237, s[60:63], s51 offen lds\n\t"
    "s_add_u32 m0, s58, 1024\n\t"
    "s_add_u32 s51, s18, s52\n\t"
    "buffer_load_dwordx4 v237, s[60:63], s51 offen lds\n\t"
    "s_mov_b32 s44, s30\n\t"
    "s_and_b32 s45, s31, 0xffff\n\t"
    "s_mov_b32 s46, 6144\n\t"
    "s_mov_b32 s47, 0x20000\n\t"
    "s_lshl_b32 s52, %1, 10\n\t"
    "s_add_u32 m0, s58, 2048\n\t"
    "s_add_u32 s51, s52, 0\n\t"
    "buffer_load_dwordx4 v233, s[44:47], s51 offen lds\n\t"
    "s_add_u32 m0, s58, 3072\n\t"
    "s_add_u32 s51, s52, 4096\n\t"
    "buffer_load_dwordx4 v233, s[44:47], s51 offen lds\n\t"
    "s_mov_b32 s44, s14\n\t"
    "s_and_b32 s45, s15, 0xffff\n\t"
    "s_mov_b32 s46, 65536\n\t"
    "s_mov_b32 s47, 0x20000\n\t"
    "s_add_u32 s64, s12, 2097152\n\t"
    "s_addc_u32 s65, s13, 0\n\t"
    "s_sub_u32 s64, s64, s58\n\t"
    "s_subb_u32 s65, s65, 0\n\t"
    "s_and_b32 s65, s65, 0xffff\n\t"
    "s_add_u32 s66, s58, 66560\n\t"
    "s_mov_b32 s67, 0x20000\n\t"
    "s_add_u32 s53, s58, 65536\n\t"
    "buffer_load_dword v238, off, s[64:67], s53 sc1\n\t"
    "s_mov_b32 s50, 0xffff0000\n\t"
    "s_mov_b32 s49, 6\n\t"
    "v_add_u32 v224, s58, v233\n\t"
    "v_and_b32 v234, 7, v232\n\t"
    "v_mul_u32_u24 v234, 1040, v234\n\t"
    "v_bfe_u32 v236, v232, 3, 1\n\t"
    "v_lshl_add_u32 v234, v236, 9, v234\n\t"
    "v_lshrrev_b32 v235, 4, v232\n\t"
    "v_lshl_add_u32 v234, v235, 7, v234\n\t"
    "v_add_u32 v225, %2, v234\n\t"
    "v_add_u32 v249, s58, v234\n\t"
    "s_lshl_b32 s51, %1, 4\n\t"
    "v_add_u32 v226, s51, v225\n\t"
    "s_add_u32 s51, %1, 1\n\t"
    "s_and_b32 s51, s51, 3\n\t"
    "s_mul_i32 s51, s51, 24320\n\t"
    "s_add_u32 s51, s51, %2\n\t"
    "s_add_u32 s51, s51, 66560\n\t"
    "v_add_u32 v227, s51, v233\n\t"
    "s_add_u32 s51, %1, 2\n\t"
    "s_and_b32 s51, s51, 3\n\t"
    "s_mul_i32 s51, s51, 24320\n\t"
    "s_add_u32 s51, s51, %2\n\t"
    "s_add_u32 s51, s51, 66560\n\t"
    "v_add_u32 v228, s51, v233\n\t"
    "s_add_u32 s51, %1, 3\n\t"
    "s_and_b32 s51, s51, 3\n\t"
    "s_mul_i32 s51, s51, 24320\n\t"
    "s_add_u32 s51, s51, %2\n\t"
    "s_add_u32 s51, s51, 66560\n\t"
    "v_add_u32 v229, s51, v233\n\t"
    "s_add_u32 s51, s58, 21248\n\t"
    "v_lshl_add_u32 v230, v235, 5, s51\n\t"
    "v_lshl_add_u32 v98, v232, 5, s51\n\t"
    "v_and_b32 v234, 15, v232\n\t"
    "v_xor_b32 v234, v234, v235\n\t"
    "v_lshlrev_b32 v234, 4, v234\n\t"
    "v_lshl_add_u32 v250, v235, 8, v234\n\t"
    "s_lshl_b32 s88, %1, 11\n\t"
    "v_bfe_u32 v234, v232, 2, 2\n\t"
    "v_mul_u32_u24 v234, 1088, v234\n\t"
    "v_and_b32 v236, 3, v232\n\t"
    "v_lshl_add_u32 v234, v236, 8, v234\n\t"
    "v_xor_b32 v236, v236, v235\n\t"
    "v_lshl_add_u32 v234, v236, 4, v234\n\t"
    "s_add_u32 s51, %1, 0\n\t"
    "s_and_b32 s51, s51, 3\n\t"
    "s_lshl_b32 s51, s51, 6\n\t"
    "s_add_u32 s51, s51, s58\n\t"
    "v_add_u32 v251, s51, v234\n\t"
    "s_add_u32 s51, %1, 1\n\t"
    "s_and_b32 s51, s51, 3\n\t"
    "s_lshl_b32 s51, s51, 6\n\t"
    "s_add_u32 s51, s51, s58\n\t"
    "v_add_u32 v252, s51, v234\n\t"
    "s_add_u32 s51, %1, 2\n\t"
    "s_and_b32 s51, s51, 3\n\t"
    "s_lshl_b32 s51, s51, 6\n\t"
    "s_add_u32 s51, s51, s58\n\t"
    "v_add_u32 v253, s51, v234\n\t"
    "s_add_u32 s51, %1, 3\n\t"
    "s_and_b32 s51, s51, 3\n\t"
    "s_lshl_b32 s51, s51, 6\n\t"
    "s_add_u32 s51, s51, s58\n\t"
    "v_add_u32 v254, s51, v234\n\t"
    "s_mul_i32 s51, %1, 327680\n\t"
    "s_add_u32 s36, s12, s51\n\t"
    "s_addc_u32 s37, s13, 0\n\t"
    "s_sub_u32 s36, s36, s58\n\t"
    "s_subb_u32 s37, s37, 0\n\t"
    "s_and_b32 s37, s37, 0xffff\n\t"
    "s_add_u32 s38, s58, 327680\n\t"
    "s_mov_b32 s39, 0x20000\n\t"
    "s_mov_b32 s48, 0\n\t"
    "s_cmp_lg_u32 s16, 0\n\t"
    "s_cselect_b32 s52, 16, 128\n\t"
    "s_cselect_b32 s53, 6, 5\n\t"
    "s_cselect_b32 s90, 1024, 16\n\t"
    "s_lshr_b32 s51, 512, s16\n\t"
    "v_bfe_u32 v234, v232, 2, 2\n\t"
    "v_mul_u32_u24 v234, 24320, v234\n\t"
    "v_and_b32 v236, 3, v232\n\t"
    "v_mad_u32_u24 v234, v236, s51, v234\n\t"
    "v_mad_u32_u24 v234, v235, s52, v234\n\t"
    "s_lshl_b32 s53, %1, s53\n\t"
    "s_add_u32 s53, s53, %2\n\t"
    "s_add_u32 s53, s53, 66560\n\t"
    "v_add_u32 v96, s53, v234\n\t"
    "v_add_u32 v99, s90, v96\n\t"
    "v_mul_u32_u24 v234, 24320, v235\n\t"
    "v_lshl_add_u32 v234, v236, 8, v234\n\t"
    "v_bfe_u32 v236, v232, 2, 2\n\t"
    "v_lshl_add_u32 v234, v236, 6, v234\n\t"
    "s_lshl_b32 s51, %1, 4\n\t"
    "s_add_u32 s51, s51, %2\n\t"
    "s_add_u32 s51, s51, 68608\n\t"
    "v_add_u32 v97, s51, v234\n\t"
    "s_mov_b32 s60, s12\n\t"
    "s_and_b32 s61, s13, 0xffff\n\t"
    "s_cmp_lg_u32 s17, 0\n\t"
    "s_cselect_b32 s62, 1310720, 0\n\t"
    "v_lshlrev_b32 v239, 10, v232\n\t"
    "s_and_b32 s89, %3, 63\n\t"
    "s_lshl_b32 s89, s89, 10\n\t"
    "s_lshl_b32 s51, %1, 14\n\t"
    "s_lshl_b32 s52, %3, 2\n\t"
    "s_add_u32 s51, s51, s52\n\t"
    "s_add_u32 s91, s51, s58\n\t"
    "s_add_u32 m0, s58, 4096\n\t"
    "s_add_u32 s51, s59, 0\n\t"
    "buffer_load_dwordx4 v248, s[80:83], s51 offen lds\n\t"
    "s_add_u32 m0, s58, 5136\n\t"
    "s_add_u32 s51, s59, 1024\n\t"
    "buffer_load_dwordx4 v248, s[80:83], s51 offen lds\n\t"
    "s_add_u32 m0, s58, 6176\n\t"
    "s_add_u32 s51, s59, 2048\n\t"
    "buffer_load_dwordx4 v248, s[80:83], s51 offen lds\n\t"
    "s_add_u32 m0, s58, 7216\n\t"
    "s_add_u32 s51, s59, 3072\n\t"
    "buffer_load_dwordx4 v248, s[80:83], s51 offen lds\n\t"
    "s_add_u32 m0, s58, 8256\n\t"
    "s_add_u32 s51, s59, 8192\n\t"
    "buffer_load_dwordx4 v248, s[80:83], s51 offen lds\n\t"
    "s_add_u32 m0, s58, 9296\n\t"
    "s_add_u32 s51, s59, 9216\n\t"
    "buffer_load_dwordx4 v248, s[80:83], s51 offen lds\n\t"
    "s_add_u32 m0, s58, 10336\n\t"
    "s_add_u32 s51, s59, 10240\n\t"
    "buffer_load_dwordx4 v248, s[80:83], s51 offen lds\n\t"
    "s_add_u32 m0, s58, 11376\n\t"
    "s_add_u32 s51, s59, 11264\n\t"
    "buffer_load_dwordx4 v248, s[80:83], s51 offen lds\n\t"
    "s_add_u32 m0, s58, 12672\n\t"
    "s_add_u32 s51, s59, 4096\n\t"
    "buffer_load_dwordx4 v248, s[80:83], s51 offen lds\n\t"
    "s_add_u32 m0, s58, 13712\n\t"
    "s_add_u32 s51, s59, 5120\n\t"
    "buffer_load_dwordx4 v248, s[80:83], s51 offen lds\n\t"
    "s_add_u32 m0, s58, 14752\n\t"
    "s_add_u32 s51, s59, 6144\n\t"
    "buffer_load_dwordx4 v248, s[80:83], s51 offen lds\n\t"
    "s_add_u32 m0, s58, 15792\n\t"
    "s_add_u32 s51, s59, 7168\n\t"
    "buffer_load_dwordx4 v248, s[80:83], s51 offen lds\n\t"
    "s_add_u32 m0, s58, 16832\n\t"
    "s_add_u32 s51, s59, 12288\n\t"
    "buffer_load_dwordx4 v248, s[80:83], s51 offen lds\n\t"
    "s_add_u32 m0, s58, 17872\n\t"
    "s_add_u32 s51, s59, 13312\n\t"
    "buffer_load_dwordx4 v248, s[80:83], s51 offen lds\n\t"
    "s_add_u32 m0, s58, 18912\n\t"
    "s_add_u32 s51, s59, 14336\n\t"
    "buffer_load_dwordx4 v248, s[80:83], s51 offen lds\n\t"
    "s_add_u32 m0, s58, 19952\n\t"
    "s_add_u32 s51, s59, 15360\n\t"
    "buffer_load_dwordx4 v248, s[80:83], s51 offen lds\n\t"
    "s_add_u32 m0, s92, 33280\n\t"
    "s_add_u32 s51, s93, 128\n\t"
    "buffer_load_dwordx4 v255, s[40:43], s51 offen nt lds\n\t"
    "s_add_u32 m0, s92, 34320\n\t"
    "s_add_u32 s51, s93, 1152\n\t"
    "buffer_load_dwordx4 v255, s[40:43], s51 offen nt lds\n\t"
    "s_add_u32 m0, s92, 35360\n\t"
    "s_add_u32 s51, s93, 2176\n\t"
    "buffer_load_dwordx4 v255, s[40:43], s51 offen nt lds\n\t"
    "s_add_u32 m0, s92, 36400\n\t"
    "s_add_u32 s51, s93, 3200\n\t"
    "buffer_load_dwordx4 v255, s[40:43], s51 offen nt lds\n\t"
    "s_add_u32 m0, s92, 37440\n\t"
    "s_add_u32 s51, s93, 4224\n\t"
    "buffer_load_dwordx4 v255, s[40:43], s51 offen nt lds\n\t"
    "s_add_u32 m0, s92, 38480\n\t"
    "s_add_u32 s51, s93, 5248\n\t"
    "buffer_load_dwordx4 v255, s[40:43], s51 offen nt lds\n\t"
    "s_add_u32 m0, s92, 39520\n\t"
    "s_add_u32 s51, s93, 6272\n\t"
    "buffer_load_dwordx4 v255, s[40:43], s51 offen nt lds\n\t"
    "s_add_u32 m0, s92, 40560\n\t"
    "s_add_u32 s51, s93, 7296\n\t"
    "buffer_load_dwordx4 v255, s[40:43], s51 offen nt lds\n\t"
    "s_waitcnt vmcnt(16)\n\t"
    "ds_read_b128 a[0:3], v249 offset:4096\n\t"
    "ds_read_b128 a[8:11], v249 offset:4112\n\t"
    "ds_read_b128 a[16:19], v249 offset:4128\n\t"
    "ds_read_b128 a[24:27], v249 offset:4144\n\t"
    "ds_read_b128 a[32:35], v249 offset:4160\n\t"
    "ds_read_b128 a[40:43], v249 offset:4176\n\t"
    "ds_read_b128 a[48:51], v249 offset:4192\n\t"
    "ds_read_b128 a[56:59], v249 offset:4208\n\t"
    "s_add_u32 m0, s58, 4096\n\t"
    "s_add_u32 s51, s59, 128\n\t"
    "s_waitcnt lgkmcnt(0)\n\t"
    "buffer_load_dwordx4 v248, s[80:83], s51 offen lds\n\t"
    "s_add_u32 m0, s58, 5136\n\t"
    "s_add_u32 s51, s59, 1152\n\t"
    "buffer_load_dwordx4 v248, s[80:83], s51 offen lds\n\t"
    "s_add_u32 m0, s58, 6176\n\t"
    "s_add_u32 s51, s59, 2176\n\t"
    "buffer_load_dwordx4 v248, s[80:83], s51 offen lds\n\t"
    "s_add_u32 m0, s58, 7216\n\t"
    "s_add_u32 s51, s59, 3200\n\t"
    "buffer_load_dwordx4 v248, s[80:83], s51 offen lds\n\t"
    "s_add_u32 m0, s58, 8256\n\t"
    "s_add_u32 s51, s59, 8320\n\t"
    "buffer_load_dwordx4 v248, s[80:83], s51 offen lds\n\t"
    "s_add_u32 m0, s58, 9296\n\t"
    "s_add_u32 s51, s59, 9344\n\t"
    "buffer_load_dwordx4 v248, s[80:83], s51 offen lds\n\t"
    "s_add_u32 m0, s58, 10336\n\t"
    "s_add_u32 s51, s59, 10368\n\t"
    "buffer_load_dwordx4 v248, s[80:83], s51 offen lds\n\t"
    "s_add_u32 m0, s58, 11376\n\t"
    "s_add_u32 s51, s59, 11392\n\t"
    "buffer_load_dwordx4 v248, s[80:83], s51 offen lds\n\t"
    "s_waitcnt vmcnt(16)\n\t"
    "ds_read_b128 a[4:7], v249 offset:12672\n\t"
    "ds_read_b128 a[12:15], v249 offset:12688\n\t"
    "ds_read_b128 a[20:23], v249 offset:12704\n\t"
    "ds_read_b128 a[28:31], v249 offset:12720\n\t"
    "ds_read_b128 a[36:39], v249 offset:12736\n\t"
    "ds_read_b128 a[44:47], v249 offset:12752\n\t"
    "ds_read_b128 a[52:55], v249 offset:12768\n\t"
    "ds_read_b128 a[60:63], v249 offset:12784\n\t"
    "s_barrier\n\t"
    "ds_read_b128 a[176:179], v225 offset:0\n\t"
    "ds_read_b128 a[180:183], v225 offset:8320\n\t"
    "ds_read_b128 a[184:187], v225 offset:16640\n\t"
    "ds_read_b128 a[188:191], v225 offset:24960\n\t"
    "ds_read_b128 a[192:195], v225 offset:16\n\t"
    "ds_read_b128 a[196:199], v225 offset:8336\n\t"
    "ds_read_b128 a[128:131], v96 offset:0\n\t"
    "s_waitcnt lgkmcnt(10)\n\t"
    "ds_read_b128 a[132:135], v99 offset:0\n\t"
    "s_waitcnt lgkmcnt(6)\n\t"
    "v_mfma_f32_16x16x32_bf16 v[192:195], a[0:3], a[176:179], 0\n\t"
    "ds_read_b128 a[200:203], v225 offset:16656\n\t"
    "s_add_u32 m0, s58, 12672\n\t"
    "s_add_u32 s51, s59, 4224\n\t"
    "buffer_load_dwordx4 v248, s[80:83], s51 offen lds\n\t"
    "v_mfma_f32_16x16x32_bf16 v[196:199], a[4:7], a[176:179], 0\n\t"
    "ds_read_b128 a[204:207], v225 offset:24976\n\t"
    "ds_read_b128 v[120:123], v97 offset:0\n\t"
    "v_mfma_f32_16x16x32_bf16 v[200:203], a[0:3], a[180:183], 0\n\t"
    "ds_read_b128 v[124:127], v97 offset:1024\n\t"
    "ds_read_b128 v[240:243], v226 offset:0\n\t"
    "ds_read_b128 v[244:247], v226 offset:8320\n\t"
    "v_mfma_f32_16x16x32_bf16 v[204:207], a[4:7], a[180:183], 0\n\t"
    "s_waitcnt lgkmcnt(10)\n\t"
    "s_add_u32 m0, s58, 13712\n\t"
    "s_add_u32 s51, s59, 5248\n\t"
    "buffer_load_dwordx4 v248, s[80:83], s51 offen lds\n\t"
    "v_mfma_f32_16x16x32_bf16 v[208:211], a[0:3], a[184:187], 0\n\t"
    "ds_read_b128 a[208:211], v225 offset:32\n\t"
    "v_mfma_f32_16x16x32_bf16 v[212:215], a[4:7], a[184:187], 0\n\t"
    "ds_read_b128 a[212:215], v225 offset:8352\n\t"
    "v_mfma_f32_16x16x32_bf16 v[216:219], a[0:3], a[188:191], 0\n\t"
    "s_add_u32 m0, s58, 14752\n\t"
    "s_add_u32 s51, s59, 6272\n\t"
    "buffer_load_dwordx4 v248, s[80:83], s51 offen lds\n\t"
    "v_mfma_f32_16x16x32_bf16 v[220:223], a[4:7], a[188:191], 0\n\t"
    "s_waitcnt lgkmcnt(10)\n\t"
    "v_mfma_f32_16x16x32_bf16 v[192:195], a[8:11], a[192:195], v[192:195]\n\t"
    "ds_read_b128 a[216:219], v225 offset:16672\n\t"
    "v_mfma_f32_16x16x32_bf16 v[196:199], a[12:15], a[192:195], v[196:199]\n\t"
    "ds_read_b128 a[220:223], v225 offset:24992\n\t"
    "s_add_u32 m0, s58, 15792\n\t"
    "s_add_u32 s51, s59, 7296\n\t"
    "buffer_load_dwordx4 v248, s[80:83], s51 offen lds\n\t"
    "v_mfma_f32_16x16x32_bf16 v[200:203], a[8:11], a[196:199], v[200:203]\n\t"
    "v_mfma_f32_16x16x32_bf16 v[204:207], a[12:15], a[196:199], v[204:207]\n\t"
    "s_waitcnt lgkmcnt(5)\n\t"
    "v_lshlrev_b32 v104, 16, v120\n\t"
    "v_lshlrev_b32 v128, 16, v240\n\t"
    "v_and_b32 v129, s50, v240\n\t"
    "v_mfma_f32_16x16x32_bf16 v[208:211], a[8:11], a[200:203], v[208:211]\n\t"
    "ds_read_b128 a[224:227], v225 offset:48\n\t"
    "s_add_u32 m0, s58, 16832\n\t"
    "s_add_u32 s51, s59, 12416\n\t"
    "buffer_load_dwordx4 v248, s[80:83], s51 offen lds\n\t"
    "v_lshlrev_b32 v130, 16, v241\n\t"
    "v_and_b32 v131, s50, v241\n\t"
    "v_mfma_f32_16x16x32_bf16 v[212:215], a[12:15], a[200:203], v[212:215]\n\t"
    "ds_read_b128 a[228:231], v225 offset:8368\n\t"
    "v_and_b32 v105, s50, v120\n\t"
    "v_lshlrev_b32 v106, 16, v121\n\t"
    "v_lshlrev_b32 v132, 16, v242\n\t"
    "v_mfma_f32_16x16x32_bf16 v[216:219], a[8:11], a[204:207], v[216:219]\n\t"
    "v_and_b32 v107, s50, v121\n\t"
    "v_lshlrev_b32 v108, 16, v122\n\t"
    "v_and_b32 v133, s50, v242\n\t"
    "v_mfma_f32_16x16x32_bf16 v[220:223], a[12:15], a[204:207], v[220:223]\n\t"
    "s_waitcnt lgkmcnt(4)\n\t"
    "s_add_u32 m0, s58, 17872\n\t"
    "s_add_u32 s51, s59, 13440\n\t"
    "buffer_load_dwordx4 v248, s[80:83], s51 offen lds\n\t"
    "v_lshlrev_b32 v134, 16, v243\n\t"
    "v_and_b32 v135, s50, v243\n\t"
    "v_mfma_f32_16x16x32_bf16 v[192:195], a[16:19], a[208:211], v[192:195]\n\t"
    "s_waitcnt vmcnt(6)\n\t"
    "ds_read_b128 a[176:179], v225 offset:16688\n\t"
    "ds_read_b128 a[64:67], v249 offset:4096\n\t"
    "ds_read_b128 v[240:243], v226 offset:16640\n\t"
    "v_and_b32 v109, s50, v122\n\t"
    "v_lshlrev_b32 v136, 16, v244\n\t"
    "v_and_b32 v137, s50, v244\n\t"
    "v_mfma_f32_16x16x32_bf16 v[196:199], a[20:23], a[208:211], v[196:199]\n\t"
    "ds_read_b128 a[180:183], v225 offset:25008\n\t"
    "ds_read_b128 a[72:75], v249 offset:4112\n\t"
    "v_lshlrev_b32 v110, 16, v123\n\t"
    "v_lshlrev_b32 v138, 16, v245\n\t"
    "v_and_b32 v139, s50, v245\n\t"
    "v_mfma_f32_16x16x32_bf16 v[200:203], a[16:19], a[212:215], v[200:203]\n\t"
    "s_add_u32 m0, s58, 18912\n\t"
    "s_add_u32 s51, s59, 14464\n\t"
    "buffer_load_dwordx4 v248, s[80:83], s51 offen lds\n\t"
    "v_lshlrev_b32 v140, 16, v246\n\t"
    "v_and_b32 v141, s50, v246\n\t"
    "v_mfma_f32_16x16x32_bf16 v[204:207], a[20:23], a[212:215], v[204:207]\n\t"
    "s_waitcnt lgkmcnt(7)\n\t"
    "ds_read_b128 a[80:83], v249 offset:4128\n\t"
    "v_and_b32 v111, s50, v123\n\t"
    "v_lshlrev_b32 v142, 16, v247\n\t"
    "v_and_b32 v143, s50, v247\n\t"
    "v_mfma_f32_16x16x32_bf16 v[208:211], a[16:19], a[216:219], v[208:211]\n\t"
    "ds_read_b128 a[184:187], v225 offset:64\n\t"
    "ds_read_b128 a[88:91], v249 offset:4144\n\t"
    "ds_read_b128 v[244:247], v226 offset:24960\n\t"
    "v_mfma_f32_16x16x32_bf16 v[212:215], a[20:23], a[216:219], v[212:215]\n\t"
    "ds_read_b128 a[188:191], v225 offset:8384\n\t"
    "s_add_u32 m0, s58, 19952\n\t"
    "s_add_u32 s51, s59, 15488\n\t"
    "buffer_load_dwordx4 v248, s[80:83], s51 offen lds\n\t"
    "v_mfma_f32_16x16x32_bf16 v[216:219], a[16:19], a[220:223], v[216:219]\n\t"
    "ds_read_b128 a[96:99], v249 offset:4160\n\t"
    "ds_read_b128 a[104:107], v249 offset:4176\n\t"
    "v_mfma_f32_16x16x32_bf16 v[220:223], a[20:23], a[220:223], v[220:223]\n\t"
    "s_waitcnt lgkmcnt(12)\n\t"
    "ds_read_b128 a[112:115], v249 offset:4192\n\t"
    "v_mfma_f32_16x16x32_bf16 v[192:195], a[24:27], a[224:227], v[192:195]\n\t"
    "ds_read_b128 a[192:195], v225 offset:16704\n\t"
    "buffer_store_dwordx4 a[128:131], v233, s[60:63], s19 offen sc1\n\t"
    "v_mfma_f32_16x16x32_bf16 v[196:199], a[28:31], a[224:227], v[196:199]\n\t"
    "s_waitcnt lgkmcnt(9)\n\t"
    "ds_read_b128 a[196:199], v225 offset:25024\n\t"
    "ds_read_b128 a[120:123], v249 offset:4208\n\t"
    "v_lshlrev_b32 v144, 16, v240\n\t"
    "v_and_b32 v145, s50, v240\n\t"
    "v_lshlrev_b32 v146, 16, v241\n\t"
    "v_mfma_f32_16x16x32_bf16 v[200:203], a[24:27], a[228:231], v[200:203]\n\t"
    "buffer_store_dwordx4 a[132:135], v233, s[60:63], s20 offen sc1\n\t"
    "v_and_b32 v147, s50, v241\n\t"
    "v_lshlrev_b32 v148, 16, v242\n\t"
    "v_and_b32 v149, s50, v242\n\t"
    "v_mfma_f32_16x16x32_bf16 v[204:207], a[28:31], a[228:231], v[204:207]\n\t"
    "ds_write_b128 v98, v[104:107] offset:0\n\t"
    "v_mfma_f32_16x16x32_bf16 v[208:211], a[24:27], a[176:179], v[208:211]\n\t"
    "ds_read_b128 a[200:203], v225 offset:80\n\t"
    "v_lshlrev_b32 v150, 16, v243\n\t"
    "v_and_b32 v151, s50, v243\n\t"
    "v_mfma_f32_16x16x32_bf16 v[212:215], a[28:31], a[176:179], v[212:215]\n\t"
    "s_waitcnt lgkmcnt(9)\n\t"
    "ds_read_b128 a[204:207], v225 offset:8400\n\t"
    "ds_read_b128 v[240:243], v226 offset:64\n\t"
    "v_lshlrev_b32 v152, 16, v244\n\t"
    "v_and_b32 v153, s50, v244\n\t"
    "v_lshlrev_b32 v154, 16, v245\n\t"
    "v_mfma_f32_16x16x32_bf16 v[216:219], a[24:27], a[180:183], v[216:219]\n\t"
    "s_waitcnt lgkmcnt(4)\n\t"
    "s_add_u32 m0, s58, 4096\n\t"
    "s_add_u32 s51, s88, 0\n\t"
    "buffer_load_dwordx4 v250, s[84:87], s51 offen lds\n\t"
    "v_and_b32 v155, s50, v245\n\t"
    "v_lshlrev_b32 v156, 16, v246\n\t"
    "v_mfma_f32_16x16x32_bf16 v[220:223], a[28:31], a[180:183], v[220:223]\n\t"
    "ds_write_b128 v98, v[108:111] offset:16\n\t"
    "v_mfma_f32_16x16x32_bf16 v[192:195], a[32:35], a[184:187], v[192:195]\n\t"
    "ds_read_b128 a[208:211], v225 offset:16720\n\t"
    "v_lshlrev_b32 v112, 16, v124\n\t"
    "v_and_b32 v113, s50, v124\n\t"
    "v_and_b32 v157, s50, v246\n\t"
    "v_mfma_f32_16x16x32_bf16 v[196:199], a[36:39], a[184:187], v[196:199]\n\t"
    "ds_read_b128 a[212:215], v225 offset:25040\n\t"
    "s_add_u32 m0, s58, 5184\n\t"
    "s_add_u32 s51, s88, 32768\n\t"
    "buffer_load_dwordx4 v250, s[84:87], s51 offen lds\n\t"
    "v_lshlrev_b32 v158, 16, v247\n\t"
    "v_and_b32 v159, s50, v247\n\t"
    "v_mfma_f32_16x16x32_bf16 v[200:203], a[32:35], a[188:191], v[200:203]\n\t"
    "ds_read_b128 v[244:247], v226 offset:8384\n\t"
    "v_lshlrev_b32 v114, 16, v125\n\t"
    "v_and_b32 v115, s50, v125\n\t"
    "v_mfma_f32_16x16x32_bf16 v[204:207], a[36:39], a[188:191], v[204:207]\n\t"
    "v_lshlrev_b32 v116, 16, v126\n\t"
    "v_and_b32 v117, s50, v126\n\t"
    "v_mfma_f32_16x16x32_bf16 v[208:211], a[32:35], a[192:195], v[208:211]\n\t"
    "ds_read_b128 a[216:219], v225 offset:96\n\t"
    "s_add_u32 m0, s58, 6272\n\t"
    "s_add_u32 s51, s88, 65536\n\t"
    "buffer_load_dwordx4 v250, s[84:87], s51 offen lds\n\t"
    "v_mfma_f32_16x16x32_bf16 v[212:215], a[36:39], a[192:195], v[212:215]\n\t"
    "ds_read_b128 a[220:223], v225 offset:8416\n\t"
    "v_lshlrev_b32 v118, 16, v127\n\t"
    "v_and_b32 v119, s50, v127\n\t"
    "v_mfma_f32_16x16x32_bf16 v[216:219], a[32:35], a[196:199], v[216:219]\n\t"
    "s_mov_b32 exec_hi, 0\n\t"
    "ds_write_b128 v98, v[112:115] offset:2048\n\t"
    "s_mov_b64 exec, -1\n\t"
    "v_mfma_f32_16x16x32_bf16 v[220:223], a[36:39], a[196:199], v[220:223]\n\t"
    "s_waitcnt lgkmcnt(7)\n\t"
    "s_add_u32 m0, s58, 7360\n\t"
    "s_add_u32 s51, s88, 98304\n\t"
    "buffer_load_dwordx4 v250, s[84:87], s51 offen lds\n\t"
    "v_lshlrev_b32 v160, 16, v240\n\t"
    "v_and_b32 v161, s50, v240\n\t"
    "v_mfma_f32_16x16x32_bf16 v[192:195], a[40:43], a[200:203], v[192:195]\n\t"
    "ds_read_b128 a[224:227], v225 offset:16736\n\t"
    "v_lshlrev_b32 v162, 16, v241\n\t"
    "v_and_b32 v163, s50, v241\n\t"
    "v_lshlrev_b32 v164, 16, v242\n\t"
    "v_mfma_f32_16x16x32_bf16 v[196:199], a[44:47], a[200:203], v[196:199]\n\t"
    "ds_read_b128 a[228:231], v225 offset:25056\n\t"
    "v_and_b32 v165, s50, v242\n\t"
    "v_lshlrev_b32 v166, 16, v243\n\t"
    "v_and_b32 v167, s50, v243\n\t"
    "v_mfma_f32_16x16x32_bf16 v[200:203], a[40:43], a[204:207], v[200:203]\n\t"
    "s_add_u32 m0, s58, 8384\n\t"
    "s_add_u32 s51, s88, 1024\n\t"
    "buffer_load_dwordx4 v250, s[84:87], s51 offen lds\n\t"
    "ds_read_b128 v[240:243], v226 offset:16704\n\t"
    "v_mfma_f32_16x16x32_bf16 v[204:207], a[44:47], a[204:207], v[204:207]\n\t"
    "s_waitcnt lgkmcnt(7)\n\t"
    "s_mov_b32 exec_hi, 0\n\t"
    "ds_write_b128 v98, v[116:119] offset:2064\n\t"
    "s_mov_b64 exec, -1\n\t"
    "v_mfma_f32_16x16x32_bf16 v[208:211], a[40:43], a[208:211], v[208:211]\n\t"
    "s_waitcnt lgkmcnt(7)\n\t"
    "ds_read_b128 a[176:179], v225 offset:112\n\t"
    "ds_read_b128 v[24:27], v230 offset:0\n\t"
    "v_lshlrev_b32 v168, 16, v244\n\t"
    "v_and_b32 v169, s50, v244\n\t"
    "v_lshlrev_b32 v170, 16, v245\n\t"
    "v_mfma_f32_16x16x32_bf16 v[212:215], a[44:47], a[208:211], v[212:215]\n\t"
    "ds_read_b128 a[180:183], v225 offset:8432\n\t"
    "s_add_u32 m0, s58, 9472\n\t"
    "s_add_u32 s51, s88, 33792\n\t"
    "buffer_load_dwordx4 v250, s[84:87], s51 offen lds\n\t"
    "v_and_b32 v171, s50, v245\n\t"
    "v_lshlrev_b32 v172, 16, v246\n\t"
    "v_mfma_f32_16x16x32_bf16 v[216:219], a[40:43], a[212:215], v[216:219]\n\t"
    "ds_read_b128 v[28:31], v230 offset:16\n\t"
    "ds_read_b128 v[56:59], v230 offset:128\n\t"
    "v_and_b32 v173, s50, v246\n\t"
    "v_lshlrev_b32 v174, 16, v247\n\t"
    "v_and_b32 v175, s50, v247\n\t"
    "v_mfma_f32_16x16x32_bf16 v[220:223], a[44:47], a[212:215], v[220:223]\n\t"
    "s_waitcnt lgkmcnt(10)\n\t"
    "ds_read_b128 v[60:63], v230 offset:144\n\t"
    "ds_read_b128 v[244:247], v226 offset:25024\n\t"
    "v_mfma_f32_16x16x32_bf16 v[192:195], a[48:51], a[216:219], v[192:195]\n\t"
    "ds_read_b128 a[184:187], v225 offset:16752\n\t"
    "s_add_u32 m0, s58, 10560\n\t"
    "s_add_u32 s51, s88, 66560\n\t"
    "buffer_load_dwordx4 v250, s[84:87], s51 offen lds\n\t"
    "v_mfma_f32_16x16x32_bf16 v[196:199], a[52:55], a[216:219], v[196:199]\n\t"
    "ds_read_b128 a[188:191], v225 offset:25072\n\t"
    "ds_read_b128 v[88:91], v230 offset:256\n\t"
    "v_mfma_f32_16x16x32_bf16 v[200:203], a[48:51], a[220:223], v[200:203]\n\t"
    "s_waitcnt lgkmcnt(10)\n\t"
    "ds_read_b128 v[92:95], v230 offset:272\n\t"
    "ds_read_b128 v[120:123], v230 offset:384\n\t"
    "v_mfma_f32_16x16x32_bf16 v[204:207], a[52:55], a[220:223], v[204:207]\n\t"
    "s_waitcnt vmcnt(7) lgkmcnt(0)\n\t"
    "v_readfirstlane_b32 s56, v238\n\t"
    "s_barrier\n\t"
    "s_nop 1\n\t"
    "s_mul_i32 s56, s56, 1664525\n\t"
    "s_add_u32 s56, s56, 1013904223\n\t"
    "v_mov_b32 v238, s56\n\t"
    "s_mov_b64 exec, 0xffff\n\t"
    "buffer_store_dword v238, v239, s[64:67], s91 offen sc1\n\t"
    "s_mov_b64 exec, -1\n\t"
    "v_mfma_f32_16x16x32_bf16 v[208:211], a[48:51], a[224:227], v[208:211]\n\t"
    "ds_read_b128 a[192:195], v225 offset:33280\n\t"
    "ds_read_b128 a[68:71], v249 offset:12672\n\t"
    "v_lshlrev_b32 v176, 16, v240\n\t"
    "v_and_b32 v177, s50, v240\n\t"
    "v_lshlrev_b32 v178, 16, v241\n\t"
    "v_mfma_f32_16x16x32_bf16 v[212:215], a[52:55], a[224:227], v[212:215]\n\t"
    "ds_read_b128 a[196:199], v225 offset:41600\n\t"
    "ds_read_b128 a[76:79], v249 offset:12688\n\t"
    "v_and_b32 v179, s50, v241\n\t"
    "v_lshlrev_b32 v180, 16, v242\n\t"
    "v_and_b32 v181, s50, v242\n\t"
    "v_mfma_f32_16x16x32_bf16 v[216:219], a[48:51], a[228:231], v[216:219]\n\t"
    "ds_read_b128 a[84:87], v249 offset:12704\n\t"
    "ds_read_b128 a[92:95], v249 offset:12720\n\t"
    "v_lshlrev_b32 v182, 16, v243\n\t"
    "v_and_b32 v183, s50, v243\n\t"
    "v_mfma_f32_16x16x32_bf16 v[220:223], a[52:55], a[228:231], v[220:223]\n\t"
    "ds_read_b128 a[100:103], v249 offset:12736\n\t"
    "v_mfma_f32_16x16x32_bf16 v[192:195], a[56:59], a[176:179], v[192:195]\n\t"
    "ds_read_b128 a[200:203], v225 offset:49920\n\t"
    "ds_read_b128 a[108:111], v249 offset:12752\n\t"
    "v_add_f32 v24, 1.0, v24\n\t"
    "v_lshlrev_b32 v184, 16, v244\n\t"
    "v_and_b32 v185, s50, v244\n\t"
    "v_mfma_f32_16x16x32_bf16 v[196:199], a[60:63], a[176:179], v[196:199]\n\t"
    "ds_read_b128 a[204:207], v225 offset:58240\n\t"
    "ds_read_b128 a[116:119], v249 offset:12768\n\t"
    "v_add_f32 v25, 1.0, v25\n\t"
    "v_lshlrev_b32 v186, 16, v245\n\t"
    "v_and_b32 v187, s50, v245\n\t"
    "v_mfma_f32_16x16x32_bf16 v[200:203], a[56:59], a[180:183], v[200:203]\n\t"
    "ds_read_b128 a[124:127], v249 offset:12784\n\t"
    "s_add_u32 m0, s58, 11648\n\t"
    "s_add_u32 s51, s88, 99328\n\t"
    "buffer_load_dwordx4 v250, s[84:87], s51 offen lds\n\t"
    "v_lshlrev_b32 v188, 16, v246\n\t"
    "v_and_b32 v189, s50, v246\n\t"
    "v_mfma_f32_16x16x32_bf16 v[204:207], a[60:63], a[180:183], v[204:207]\n\t"
    "v_add_f32 v26, 1.0, v26\n\t"
    "v_add_f32 v27, 1.0, v27\n\t"
    "v_lshlrev_b32 v190, 16, v247\n\t"
    "v_mfma_f32_16x16x32_bf16 v[208:211], a[56:59], a[184:187], v[208:211]\n\t"
    "ds_read_b128 a[208:211], v225 offset:33296\n\t"
    "v_add_f32 v28, 1.0, v28\n\t"
    "v_add_f32 v29, 1.0, v29\n\t"
    "v_and_b32 v191, s50, v247\n\t"
    "v_mfma_f32_16x16x32_bf16 v[212:215], a[60:63], a[184:187], v[212:215]\n\t"
    "ds_read_b128 a[212:215], v225 offset:41616\n\t"
    "v_add_f32 v30, 1.0, v30\n\t"
    "v_add_f32 v31, 1.0, v31\n\t"
    "v_mfma_f32_16x16x32_bf16 v[216:219], a[56:59], a[188:191], v[216:219]\n\t"
    "ds_read_b128 v[124:127], v230 offset:400\n\t"
    "v_add_f32 v56, 1.0, v56\n\t"
    "v_add_f32 v57, 1.0, v57\n\t"
    "v_mfma_f32_16x16x32_bf16 v[220:223], a[60:63], a[188:191], v[220:223]\n\t"
    "s_waitcnt lgkmcnt(3)\n\t"
    "s_add_u32 m0, s58, 12672\n\t"
    "s_add_u32 s51, s88, 8192\n\t"
    "buffer_load_dwordx4 v250, s[84:87], s51 offen lds\n\t"
    "v_mfma_f32_16x16x32_bf16 v[192:195], a[64:67], a[192:195], v[192:195]\n\t"
    "ds_read_b128 a[216:219], v225 offset:49936\n\t"
    "v_add_f32 v58, 1.0, v58\n\t"
    "v_add_f32 v59, 1.0, v59\n\t"
    "v_mfma_f32_16x16x32_bf16 v[196:199], a[68:71], a[192:195], v[196:199]\n\t"
    "ds_read_b128 a[220:223], v225 offset:58256\n\t"
    "v_add_f32 v60, 1.0, v60\n\t"
    "v_add_f32 v61, 1.0, v61\n\t"
    "v_mfma_f32_16x16x32_bf16 v[200:203], a[64:67], a[196:199], v[200:203]\n\t"
    "s_add_u32 m0, s58, 13760\n\t"
    "s_add_u32 s51, s88, 40960\n\t"
    "buffer_load_dwordx4 v250, s[84:87], s51 offen lds\n\t"
    "v_mfma_f32_16x16x32_bf16 v[204:207], a[68:71], a[196:199], v[204:207]\n\t"
    "v_add_f32 v62, 1.0, v62\n\t"
    "v_add_f32 v63, 1.0, v63\n\t"
    "v_mfma_f32_16x16x32_bf16 v[208:211], a[64:67], a[200:203], v[208:211]\n\t"
    "ds_read_b128 a[224:227], v225 offset:33312\n\t"
    "v_add_f32 v88, 1.0, v88\n\t"
    "v_add_f32 v89, 1.0, v89\n\t"
    "v_mfma_f32_16x16x32_bf16 v[212:215], a[68:71], a[200:203], v[212:215]\n\t"
    "ds_read_b128 a[228:231], v225 offset:41632\n\t"
    "s_add_u32 m0, s58, 14848\n\t"
    "s_add_u32 s51, s88, 73728\n\t"
    "buffer_load_dwordx4 v250, s[84:87], s51 offen lds\n\t"
    "v_mfma_f32_16x16x32_bf16 v[216:219], a[64:67], a[204:207], v[216:219]\n\t"
    "v_add_f32 v90, 1.0, v90\n\t"
    "v_add_f32 v91, 1.0, v91\n\t"
    "v_mfma_f32_16x16x32_bf16 v[220:223], a[68:71], a[204:207], v[220:223]\n\t"
    "s_waitcnt lgkmcnt(5)\n\t"
    "v_add_f32 v92, 1.0, v92\n\t"
    "v_add_f32 v93, 1.0, v93\n\t"
    "v_mfma_f32_16x16x32_bf16 v[192:195], a[72:75], a[208:211], v[192:195]\n\t"
    "ds_read_b128 a[176:179], v225 offset:49952\n\t"
    "s_add_u32 m0, s58, 15936\n\t"
    "s_add_u32 s51, s88, 106496\n\t"
    "buffer_load_dwordx4 v250, s[84:87], s51 offen lds\n\t"
    "v_mfma_f32_16x16x32_bf16 v[196:199], a[76:79], a[208:211], v[196:199]\n\t"
    "ds_read_b128 a[180:183], v225 offset:58272\n\t"
    "v_add_f32 v94, 1.0, v94\n\t"
    "v_add_f32 v95, 1.0, v95\n\t"
    "v_mfma_f32_16x16x32_bf16 v[200:203], a[72:75], a[212:215], v[200:203]\n\t"
    "v_add_f32 v120, 1.0, v120\n\t"
    "v_add_f32 v121, 1.0, v121\n\t"
    "v_mfma_f32_16x16x32_bf16 v[204:207], a[76:79], a[212:215], v[204:207]\n\t"
    "s_waitcnt lgkmcnt(4)\n\t"
    "s_add_u32 m0, s58, 16960\n\t"
    "s_add_u32 s51, s88, 9216\n\t"
    "buffer_load_dwordx4 v250, s[84:87], s51 offen lds\n\t"
    "v_mfma_f32_16x16x32_bf16 v[208:211], a[72:75], a[216:219], v[208:211]\n\t"
    "s_waitcnt vmcnt(10)\n\t"
    "ds_read_b128 a[184:187], v225 offset:33328\n\t"
    "ds_read_b128 a[0:3], v251 offset:4096\n\t"
    "v_add_f32 v122, 1.0, v122\n\t"
    "v_mfma_f32_16x16x32_bf16 v[212:215], a[76:79], a[216:219], v[212:215]\n\t"
    "ds_read_b128 a[188:191], v225 offset:41648\n\t"
    "ds_read_b128 a[24:27], v252 offset:4096\n\t"
    "v_add_f32 v123, 1.0, v123\n\t"
    "v_mfma_f32_16x16x32_bf16 v[216:219], a[72:75], a[220:223], v[216:219]\n\t"
    "s_add_u32 m0, s58, 18048\n\t"
    "s_add_u32 s51, s88, 41984\n\t"
    "buffer_load_dwordx4 v250, s[84:87], s51 offen lds\n\t"
    "v_mfma_f32_16x16x32_bf16 v[220:223], a[76:79], a[220:223], v[220:223]\n\t"
    "s_waitcnt lgkmcnt(6)\n\t"
    "ds_read_b128 a[28:31], v253 offset:4096\n\t"
    "v_add_f32 v124, 1.0, v124\n\t"
    "v_mfma_f32_16x16x32_bf16 v[192:195], a[80:83], a[224:227], v[192:195]\n\t"
    "ds_read_b128 a[192:195], v225 offset:49968\n\t"
    "ds_read_b128 a[32:35], v254 offset:4096\n\t"
    "v_add_f32 v125, 1.0, v125\n\t"
    "v_mfma_f32_16x16x32_bf16 v[196:199], a[84:87], a[224:227], v[196:199]\n\t"
    "ds_read_b128 a[196:199], v225 offset:58288\n\t"
    "s_add_u32 m0, s58, 19136\n\t"
    "s_add_u32 s51, s88, 74752\n\t"
    "buffer_load_dwordx4 v250, s[84:87], s51 offen lds\n\t"
    "v_mfma_f32_16x16x32_bf16 v[200:203], a[80:83], a[228:231], v[200:203]\n\t"
    "v_add_f32 v126, 1.0, v126\n\t"
    "v_add_f32 v127, 1.0, v127\n\t"
    "v_mfma_f32_16x16x32_bf16 v[204:207], a[84:87], a[228:231], v[204:207]\n\t"
    "s_waitcnt lgkmcnt(8)\n\t"
    "v_add_u32 v230, 512, v230\n\t"
    "v_mfma_f32_16x16x32_bf16 v[208:211], a[80:83], a[176:179], v[208:211]\n\t"
    "ds_read_b128 a[200:203], v225 offset:33344\n\t"
    "s_add_u32 m0, s58, 20224\n\t"
    "s_add_u32 s51, s88, 107520\n\t"
    "buffer_load_dwordx4 v250, s[84:87], s51 offen lds\n\t"
    "v_mfma_f32_16x16x32_bf16 v[212:215], a[84:87], a[176:179], v[212:215]\n\t"
    "ds_read_b128 a[204:207], v225 offset:33360\n\t"
    "v_mfma_f32_16x16x32_bf16 v[216:219], a[80:83], a[180:183], v[216:219]\n\t"
    "v_mfma_f32_16x16x32_bf16 v[220:223], a[84:87], a[180:183], v[220:223]\n\t"
    "s_waitcnt lgkmcnt(3)\n\t"
    "s_add_u32 m0, s58, 4096\n\t"
    "s_add_u32 s51, s88, 16384\n\t"
    "buffer_load_dwordx4 v250, s[84:87], s51 offen lds\n\t"
    "v_mfma_f32_16x16x32_bf16 v[192:195], a[88:91], a[184:187], v[192:195]\n\t"
    "ds_read_b128 a[208:211], v225 offset:33376\n\t"
    "v_mfma_f32_16x16x32_bf16 v[196:199], a[92:95], a[184:187], v[196:199]\n\t"
    "ds_read_b128 a[212:215], v225 offset:33392\n\t"
    "v_mfma_f32_16x16x32_bf16 v[200:203], a[88:91], a[188:191], v[200:203]\n\t"
    "s_add_u32 m0, s58, 5184\n\t"
    "s_add_u32 s51, s88, 49152\n\t"
    "buffer_load_dwordx4 v250, s[84:87], s51 offen lds\n\t"
    "v_mfma_f32_16x16x32_bf16 v[204:207], a[92:95], a[188:191], v[204:207]\n\t"
    "v_mfma_f32_16x16x32_bf16 v[208:211], a[88:91], a[192:195], v[208:211]\n\t"
    "ds_read_b128 a[216:219], v225 offset:41664\n\t"
    "v_mfma_f32_16x16x32_bf16 v[212:215], a[92:95], a[192:195], v[212:215]\n\t"
    "s_waitcnt lgkmcnt(5)\n\t"
    "ds_read_b128 a[220:223], v225 offset:41680\n\t"
    "s_add_u32 m0, s58, 6272\n\t"
    "s_add_u32 s51, s88, 81920\n\t"
    "buffer_load_dwordx4 v250, s[84:87], s51 offen lds\n\t"
    "v_mfma_f32_16x16x32_bf16 v[216:219], a[88:91], a[196:199], v[216:219]\n\t"
    "s_waitcnt vmcnt(11)\n\t"
    "ds_read_b128 a[4:7], v251 offset:8384\n\t"
    "ds_read_b128 a[36:39], v252 offset:8384\n\t"
    "v_mfma_f32_16x16x32_bf16 v[220:223], a[92:95], a[196:199], v[220:223]\n\t"
    "s_waitcnt lgkmcnt(6)\n\t"
    "ds_read_b128 a[40:43], v253 offset:8384\n\t"
    "v_mfma_f32_16x16x32_bf16 v[192:195], a[96:99], a[200:203], v[192:195]\n\t"
    "ds_read_b128 a[224:227], v225 offset:41696\n\t"
    "s_add_u32 m0, s58, 7360\n\t"
    "s_add_u32 s51, s88, 114688\n\t"
    "buffer_load_dwordx4 v250, s[84:87], s51 offen lds\n\t"
    "v_mfma_f32_16x16x32_bf16 v[196:199], a[100:103], a[200:203], v[196:199]\n\t"
    "ds_read_b128 a[228:231], v225 offset:41712\n\t"
    "ds_read_b128 a[44:47], v254 offset:8384\n\t"
    "v_mfma_f32_16x16x32_bf16 v[192:195], a[104:107], a[204:207], v[192:195]\n\t"
    "v_mfma_f32_16x16x32_bf16 v[196:199], a[108:111], a[204:207], v[196:199]\n\t"
    "s_waitcnt lgkmcnt(8)\n\t"
    "v_mfma_f32_16x16x32_bf16 v[192:195], a[112:115], a[208:211], v[192:195]\n\t"
    "ds_read_b128 a[176:179], v225 offset:49984\n\t"
    "v_mfma_f32_16x16x32_bf16 v[196:199], a[116:119], a[208:211], v[196:199]\n\t"
    "ds_read_b128 a[180:183], v225 offset:50000\n\t"
    "v_mfma_f32_16x16x32_bf16 v[192:195], a[120:123], a[212:215], v[192:195]\n\t"
    "s_waitcnt lgkmcnt(2)\n\t"
    "s_add_u32 m0, s58, 8384\n\t"
    "s_add_u32 s51, s88, 17408\n\t"
    "buffer_load_dwordx4 v250, s[84:87], s51 offen lds\n\t"
    "v_mfma_f32_16x16x32_bf16 v[196:199], a[124:127], a[212:215], v[196:199]\n\t"
    "v_mfma_f32_16x16x32_bf16 v[200:203], a[96:99], a[216:219], v[200:203]\n\t"
    "ds_read_b128 a[184:187], v225 offset:50016\n\t"
    "v_mfma_f32_16x16x32_bf16 v[204:207], a[100:103], a[216:219], v[204:207]\n\t"
    "ds_read_b128 a[188:191], v225 offset:50032\n\t"
    "s_add_u32 m0, s58, 9472\n\t"
    "s_add_u32 s51, s88, 50176\n\t"
    "buffer_load_dwordx4 v250, s[84:87], s51 offen lds\n\t"
    "v_mfma_f32_16x16x32_bf16 v[200:203], a[104:107], a[220:223], v[200:203]\n\t"
    "v_cvt_pk_bf16_f32 v192, v192, v193\n\t"
    "v_cvt_pk_bf16_f32 v193, v194, v195\n\t"
    "v_mfma_f32_16x16x32_bf16 v[204:207], a[108:111], a[220:223], v[204:207]\n\t"
    "v_cvt_pk_bf16_f32 v194, v196, v197\n\t"
    "v_cvt_pk_bf16_f32 v195, v198, v199\n\t"
    "v_mfma_f32_16x16x32_bf16 v[200:203], a[112:115], a[224:227], v[200:203]\n\t"
    "ds_read_b128 a[192:195], v225 offset:58304\n\t"
    "s_add_u32 m0, s58, 10560\n\t"
    "s_add_u32 s51, s88, 82944\n\t"
    "buffer_load_dwordx4 v250, s[84:87], s51 offen lds\n\t"
    "v_mfma_f32_16x16x32_bf16 v[204:207], a[116:119], a[224:227], v[204:207]\n\t"
    "s_waitcnt vmcnt(11)\n\t"
    "ds_read_b128 a[196:199], v225 offset:58320\n\t"
    "ds_read_b128 a[8:11], v251 offset:12672\n\t"
    "v_mfma_f32_16x16x32_bf16 v[200:203], a[120:123], a[228:231], v[200:203]\n\t"
    "ds_write_b128 v224, v[192:195] offset:0\n\t"
    "v_mfma_f32_16x16x32_bf16 v[204:207], a[124:127], a[228:231], v[204:207]\n\t"
    "s_waitcnt lgkmcnt(6)\n\t"
    "s_add_u32 m0, s58, 11648\n\t"
    "s_add_u32 s51, s88, 115712\n\t"
    "buffer_load_dwordx4 v250, s[84:87], s51 offen lds\n\t"
    "v_mfma_f32_16x16x32_bf16 v[208:211], a[96:99], a[176:179], v[208:211]\n\t"
    "ds_read_b128 a[200:203], v225 offset:58336\n\t"
    "ds_read_b128 a[48:51], v252 offset:12672\n\t"
    "v_mfma_f32_16x16x32_bf16 v[212:215], a[100:103], a[176:179], v[212:215]\n\t"
    "ds_read_b128 a[204:207], v225 offset:58352\n\t"
    "ds_read_b128 a[52:55], v253 offset:12672\n\t"
    "v_mfma_f32_16x16x32_bf16 v[208:211], a[104:107], a[180:183], v[208:211]\n\t"
    "ds_read_b128 a[56:59], v254 offset:12672\n\t"
    "v_cvt_pk_bf16_f32 v200, v200, v201\n\t"
    "v_cvt_pk_bf16_f32 v201, v202, v203\n\t"
    "v_mfma_f32_16x16x32_bf16 v[212:215], a[108:111], a[180:183], v[212:215]\n\t"
    "s_waitcnt lgkmcnt(9)\n\t"
    "v_cvt_pk_bf16_f32 v202, v204, v205\n\t"
    "v_cvt_pk_bf16_f32 v203, v206, v207\n\t"
    "v_mfma_f32_16x16x32_bf16 v[208:211], a[112:115], a[184:187], v[208:211]\n\t"
    "ds_write_b128 v224, v[200:203] offset:1024\n\t"
    "v_mfma_f32_16x16x32_bf16 v[212:215], a[116:119], a[184:187], v[212:215]\n\t"
    "v_mfma_f32_16x16x32_bf16 v[208:211], a[120:123], a[188:191], v[208:211]\n\t"
    "v_mfma_f32_16x16x32_bf16 v[212:215], a[124:127], a[188:191], v[212:215]\n\t"
    "s_waitcnt lgkmcnt(1)\n\t"
    "s_add_u32 m0, s58, 12672\n\t"
    "s_add_u32 s51, s88, 24576\n\t"
    "buffer_load_dwordx4 v250, s[84:87], s51 offen lds\n\t"
    "v_mfma_f32_16x16x32_bf16 v[216:219], a[96:99], a[192:195], v[216:219]\n\t"
    "s_waitcnt vmcnt(9)\n\t"
    "ds_read_b128 a[12:15], v251 offset:16960\n\t"
    "ds_read_b128 a[60:63], v252 offset:16960\n\t"
    "v_mfma_f32_16x16x32_bf16 v[220:223], a[100:103], a[192:195], v[220:223]\n\t"
    "ds_read_b128 a[64:67], v253 offset:16960\n\t"
    "ds_read_b128 a[68:71], v254 offset:16960\n\t"
    "v_mfma_f32_16x16x32_bf16 v[216:219], a[104:107], a[196:199], v[216:219]\n\t"
    "v_cvt_pk_bf16_f32 v208, v208, v209\n\t"
    "v_cvt_pk_bf16_f32 v209, v210, v211\n\t"
    "v_mfma_f32_16x16x32_bf16 v[220:223], a[108:111], a[196:199], v[220:223]\n\t"
    "v_cvt_pk_bf16_f32 v210, v212, v213\n\t"
    "v_cvt_pk_bf16_f32 v211, v214, v215\n\t"
    "v_mfma_f32_16x16x32_bf16 v[216:219], a[112:115], a[200:203], v[216:219]\n\t"
    "ds_write_b128 v224, v[208:211] offset:2048\n\t"
    "v_mfma_f32_16x16x32_bf16 v[220:223], a[116:119], a[200:203], v[220:223]\n\t"
    "s_add_u32 m0, s58, 13760\n\t"
    "s_add_u32 s51, s88, 57344\n\t"
    "buffer_load_dwordx4 v250, s[84:87], s51 offen lds\n\t"
    "v_mfma_f32_16x16x32_bf16 v[216:219], a[120:123], a[204:207], v[216:219]\n\t"
    "v_mfma_f32_16x16x32_bf16 v[220:223], a[124:127], a[204:207], v[220:223]\n\t"
    "v_mfma_f32_16x16x32_bf16 v[0:3], a[0:3], v[192:195], v[24:27]\n\t"
    "s_add_u32 m0, s58, 14848\n\t"
    "s_add_u32 s51, s88, 90112\n\t"
    "buffer_load_dwordx4 v250, s[84:87], s51 offen lds\n\t"
    "v_mfma_f32_16x16x32_bf16 v[4:7], a[4:7], v[192:195], v[28:31]\n\t"
    "v_mfma_f32_16x16x32_bf16 v[32:35], a[8:11], v[192:195], v[56:59]\n\t"
    "s_waitcnt lgkmcnt(1)\n\t"
    "v_cvt_pk_bf16_f32 v216, v216, v217\n\t"
    "v_cvt_pk_bf16_f32 v217, v218, v219\n\t"
    "v_mfma_f32_16x16x32_bf16 v[36:39], a[12:15], v[192:195], v[60:63]\n\t"
    "v_cvt_pk_bf16_f32 v218, v220, v221\n\t"
    "v_cvt_pk_bf16_f32 v219, v222, v223\n\t"
    "v_mfma_f32_16x16x32_bf16 v[8:11], a[0:3], v[200:203], v[24:27]\n\t"
    "ds_write_b128 v224, v[216:219] offset:3072\n\t"
    "v_mfma_f32_16x16x32_bf16 v[12:15], a[4:7], v[200:203], v[28:31]\n\t"
    "s_add_u32 m0, s58, 15936\n\t"
    "s_add_u32 s51, s88, 122880\n\t"
    "buffer_load_dwordx4 v250, s[84:87], s51 offen lds\n\t"
    "v_mfma_f32_16x16x32_bf16 v[40:43], a[8:11], v[200:203], v[56:59]\n\t"
    "s_waitcnt vmcnt(8)\n\t"
    "ds_read_b128 a[16:19], v251 offset:4096\n\t"
    "ds_read_b128 a[72:75], v252 offset:4096\n\t"
    "v_mfma_f32_16x16x32_bf16 v[44:47], a[12:15], v[200:203], v[60:63]\n\t"
    "s_waitcnt lgkmcnt(2)\n\t"
    "s_barrier\n\t"
    "v_mfma_f32_16x16x32_bf16 v[16:19], a[0:3], v[208:211], v[24:27]\n\t"
    "ds_read_b128 a[128:131], v227 offset:0\n\t"
    "ds_read_b128 a[144:147], v228 offset:0\n\t"
    "v_mfma_f32_16x16x32_bf16 v[20:23], a[4:7], v[208:211], v[28:31]\n\t"
    "ds_read_b128 a[160:163], v229 offset:0\n\t"
    "ds_read_b128 a[132:135], v227 offset:1024\n\t"
    "v_mfma_f32_16x16x32_bf16 v[48:51], a[8:11], v[208:211], v[56:59]\n\t"
    "ds_read_b128 a[148:151], v228 offset:1024\n\t"
    "ds_read_b128 a[164:167], v229 offset:1024\n\t"
    "v_mfma_f32_16x16x32_bf16 v[52:55], a[12:15], v[208:211], v[60:63]\n\t"
    "ds_read_b128 a[136:139], v227 offset:2048\n\t"
    "ds_read_b128 a[152:155], v228 offset:2048\n\t"
    "v_mfma_f32_16x16x32_bf16 v[24:27], a[0:3], v[216:219], v[24:27]\n\t"
    "ds_read_b128 a[168:171], v229 offset:2048\n\t"
    "ds_read_b128 a[140:143], v227 offset:3072\n\t"
    "v_mfma_f32_16x16x32_bf16 v[28:31], a[4:7], v[216:219], v[28:31]\n\t"
    "ds_read_b128 a[156:159], v228 offset:3072\n\t"
    "ds_read_b128 a[172:175], v229 offset:3072\n\t"
    "v_mfma_f32_16x16x32_bf16 v[56:59], a[8:11], v[216:219], v[56:59]\n\t"
    "s_add_u32 m0, s58, 16960\n\t"
    "s_add_u32 s51, s88, 25600\n\t"
    "buffer_load_dwordx4 v250, s[84:87], s51 offen lds\n\t"
    "v_mfma_f32_16x16x32_bf16 v[60:63], a[12:15], v[216:219], v[60:63]\n\t"
    "s_waitcnt lgkmcnt(9)\n\t"
    "ds_read_b128 a[76:79], v253 offset:4096\n\t"
    "v_mfma_f32_16x16x32_bf16 v[0:3], a[24:27], a[128:131], v[0:3]\n\t"
    "ds_read_b128 a[80:83], v254 offset:4096\n\t"
    "v_mfma_f32_16x16x32_bf16 v[0:3], a[28:31], a[144:147], v[0:3]\n\t"
    "s_add_u32 m0, s58, 18048\n\t"
    "s_add_u32 s51, s88, 58368\n\t"
    "buffer_load_dwordx4 v250, s[84:87], s51 offen lds\n\t"
    "v_mfma_f32_16x16x32_bf16 v[0:3], a[32:35], a[160:163], v[0:3]\n\t"
    "buffer_load_dwordx4 v[196:199], v224, s[64:67], s89 offen sc1\n\t"
    "v_mfma_f32_16x16x32_bf16 v[4:7], a[36:39], a[128:131], v[4:7]\n\t"
    "s_waitcnt vmcnt(7)\n\t"
    "ds_read_b128 a[20:23], v251 offset:8384\n\t"
    "ds_read_b128 a[84:87], v252 offset:8384\n\t"
    "v_mfma_f32_16x16x32_bf16 v[4:7], a[40:43], a[144:147], v[4:7]\n\t"
    "s_add_u32 m0, s58, 19136\n\t"
    "s_add_u32 s51, s88, 91136\n\t"
    "buffer_load_dwordx4 v250, s[84:87], s51 offen lds\n\t"
    "v_mfma_f32_16x16x32_bf16 v[4:7], a[44:47], a[160:163], v[4:7]\n\t"
    "s_waitcnt lgkmcnt(10)\n\t"
    "ds_read_b128 a[88:91], v253 offset:8384\n\t"
    "v_mfma_f32_16x16x32_bf16 v[8:11], a[24:27], a[132:135], v[8:11]\n\t"
    "ds_read_b128 a[92:95], v254 offset:8384\n\t"
    "v_mfma_f32_16x16x32_bf16 v[8:11], a[28:31], a[148:151], v[8:11]\n\t"
    "s_add_u32 m0, s58, 20224\n\t"
    "s_add_u32 s51, s88, 123904\n\t"
    "buffer_load_dwordx4 v250, s[84:87], s51 offen lds\n\t"
    "v_mfma_f32_16x16x32_bf16 v[8:11], a[32:35], a[164:167], v[8:11]\n\t"
    "v_mul_f32 v232, v128, v0\n\t"
    "v_mul_f32 v233, v129, v1\n\t"
    "v_mfma_f32_16x16x32_bf16 v[12:15], a[36:39], a[132:135], v[12:15]\n\t"
    "v_mul_f32 v234, v130, v2\n\t"
    "v_mul_f32 v235, v131, v3\n\t"
    "v_mfma_f32_16x16x32_bf16 v[12:15], a[40:43], a[148:151], v[12:15]\n\t"
    "v_cvt_pk_bf16_f32 v232, v232, v233\n\t"
    "v_mul_f32 v236, v132, v4\n\t"
    "v_mfma_f32_16x16x32_bf16 v[12:15], a[44:47], a[164:167], v[12:15]\n\t"
    "s_waitcnt lgkmcnt(9)\n\t"
    "v_mul_f32 v237, v133, v5\n\t"
    "v_cvt_pk_bf16_f32 v233, v234, v235\n\t"
    "v_mfma_f32_16x16x32_bf16 v[16:19], a[24:27], a[136:139], v[16:19]\n\t"
    "v_mul_f32 v238, v134, v6\n\t"
    "v_mul_f32 v239, v135, v7\n\t"
    "v_mfma_f32_16x16x32_bf16 v[16:19], a[28:31], a[152:155], v[16:19]\n\t"
    "v_cvt_pk_bf16_f32 v234, v236, v237\n\t"
    "v_cvt_pk_bf16_f32 v235, v238, v239\n\t"
    "v_mfma_f32_16x16x32_bf16 v[16:19], a[32:35], a[168:171], v[16:19]\n\t"
    "ds_write_b128 v226, v[232:235] offset:0\n\t"
    "v_mfma_f32_16x16x32_bf16 v[20:23], a[36:39], a[136:139], v[20:23]\n\t"
    "v_mul_f32 v240, v136, v8\n\t"
    "v_mul_f32 v241, v137, v9\n\t"
    "v_mfma_f32_16x16x32_bf16 v[20:23], a[40:43], a[152:155], v[20:23]\n\t"
    "v_mul_f32 v242, v138, v10\n\t"
    "v_mul_f32 v243, v139, v11\n\t"
    "v_mfma_f32_16x16x32_bf16 v[20:23], a[44:47], a[168:171], v[20:23]\n\t"
    "s_waitcnt lgkmcnt(7)\n\t"
    "v_cvt_pk_bf16_f32 v240, v240, v241\n\t"
    "v_mul_f32 v244, v140, v12\n\t"
    "v_mfma_f32_16x16x32_bf16 v[24:27], a[24:27], a[140:143], v[24:27]\n\t"
    "v_mul_f32 v245, v141, v13\n\t"
    "v_cvt_pk_bf16_f32 v241, v242, v243\n\t"
    "v_mfma_f32_16x16x32_bf16 v[24:27], a[28:31], a[156:159], v[24:27]\n\t"
    "v_mul_f32 v246, v142, v14\n\t"
    "v_mul_f32 v247, v143, v15\n\t"
    "v_mfma_f32_16x16x32_bf16 v[24:27], a[32:35], a[172:175], v[24:27]\n\t"
    "v_cvt_pk_bf16_f32 v242, v244, v245\n\t"
    "v_cvt_pk_bf16_f32 v243, v246, v247\n\t"
    "v_mfma_f32_16x16x32_bf16 v[28:31], a[36:39], a[140:143], v[28:31]\n\t"
    "ds_write_b128 v226, v[240:243] offset:8320\n\t"
    "v_mfma_f32_16x16x32_bf16 v[28:31], a[40:43], a[156:159], v[28:31]\n\t"
    "v_mul_f32 v232, v144, v16\n\t"
    "v_mul_f32 v233, v145, v17\n\t"
    "v_mfma_f32_16x16x32_bf16 v[28:31], a[44:47], a[172:175], v[28:31]\n\t"
    "v_mul_f32 v234, v146, v18\n\t"
    "v_mul_f32 v235, v147, v19\n\t"
    "v_mfma_f32_16x16x32_bf16 v[32:35], a[48:51], a[128:131], v[32:35]\n\t"
    "v_cvt_pk_bf16_f32 v232, v232, v233\n\t"
    "v_mul_f32 v236, v148, v20\n\t"
    "v_mfma_f32_16x16x32_bf16 v[32:35], a[52:55], a[144:147], v[32:35]\n\t"
    "s_waitcnt vmcnt(5)\n\t"
    "ds_read_b128 a[96:99], v251 offset:12672\n\t"
    "ds_read_b128 a[100:103], v252 offset:12672\n\t"
    "v_mul_f32 v237, v149, v21\n\t"
    "v_mfma_f32_16x16x32_bf16 v[32:35], a[56:59], a[160:163], v[32:35]\n\t"
    "ds_read_b128 a[104:107], v253 offset:12672\n\t"
    "ds_read_b128 a[108:111], v254 offset:12672\n\t"
    "v_cvt_pk_bf16_f32 v233, v234, v235\n\t"
    "v_mfma_f32_16x16x32_bf16 v[36:39], a[60:63], a[128:131], v[36:39]\n\t"
    "v_mul_f32 v238, v150, v22\n\t"
    "v_mul_f32 v239, v151, v23\n\t"
    "v_mfma_f32_16x16x32_bf16 v[36:39], a[64:67], a[144:147], v[36:39]\n\t"
    "v_cvt_pk_bf16_f32 v234, v236, v237\n\t"
    "v_cvt_pk_bf16_f32 v235, v238, v239\n\t"
    "v_mfma_f32_16x16x32_bf16 v[36:39], a[68:71], a[160:163], v[36:39]\n\t"
    "ds_write_b128 v226, v[232:235] offset:16640\n\t"
    "v_mfma_f32_16x16x32_bf16 v[40:43], a[48:51], a[132:135], v[40:43]\n\t"
    "v_mul_f32 v240, v152, v24\n\t"
    "v_mul_f32 v241, v153, v25\n\t"
    "v_mfma_f32_16x16x32_bf16 v[40:43], a[52:55], a[148:151], v[40:43]\n\t"
    "v_mul_f32 v242, v154, v26\n\t"
    "v_mul_f32 v243, v155, v27\n\t"
    "v_mfma_f32_16x16x32_bf16 v[40:43], a[56:59], a[164:167], v[40:43]\n\t"
    "v_cvt_pk_bf16_f32 v240, v240, v241\n\t"
    "v_mul_f32 v244, v156, v28\n\t"
    "v_mfma_f32_16x16x32_bf16 v[44:47], a[60:63], a[132:135], v[44:47]\n\t"
    "v_mul_f32 v245, v157, v29\n\t"
    "v_cvt_pk_bf16_f32 v241, v242, v243\n\t"
    "v_mfma_f32_16x16x32_bf16 v[44:47], a[64:67], a[148:151], v[44:47]\n\t"
    "v_mul_f32 v246, v158, v30\n\t"
    "v_mul_f32 v247, v159, v31\n\t"
    "v_mfma_f32_16x16x32_bf16 v[44:47], a[68:71], a[164:167], v[44:47]\n\t"
    "v_cvt_pk_bf16_f32 v242, v244, v245\n\t"
    "v_cvt_pk_bf16_f32 v243, v246, v247\n\t"
    "v_mfma_f32_16x16x32_bf16 v[48:51], a[48:51], a[136:139], v[48:51]\n\t"
    "ds_write_b128 v226, v[240:243] offset:24960\n\t"
    "v_mfma_f32_16x16x32_bf16 v[48:51], a[52:55], a[152:155], v[48:51]\n\t"
    "v_mul_f32 v232, v160, v32\n\t"
    "v_mul_f32 v233, v161, v33\n\t"
    "v_mfma_f32_16x16x32_bf16 v[48:51], a[56:59], a[168:171], v[48:51]\n\t"
    "v_mul_f32 v234, v162, v34\n\t"
    "v_mul_f32 v235, v163, v35\n\t"
    "v_mfma_f32_16x16x32_bf16 v[52:55], a[60:63], a[136:139], v[52:55]\n\t"
    "v_cvt_pk_bf16_f32 v232, v232, v233\n\t"
    "v_mul_f32 v236, v164, v36\n\t"
    "v_mfma_f32_16x16x32_bf16 v[52:55], a[64:67], a[152:155], v[52:55]\n\t"
    "v_mul_f32 v237, v165, v37\n\t"
    "v_cvt_pk_bf16_f32 v233, v234, v235\n\t"
    "v_mfma_f32_16x16x32_bf16 v[52:55], a[68:71], a[168:171], v[52:55]\n\t"
    "s_waitcnt vmcnt(2)\n\t"
    "s_mov_b32 s57, 200000\n\t"
    "v_mov_b32 v204, v196\n\t"
    "s_branch Lchk%=\n\t"
    "Lpoll%=:\n\t"
    "buffer_load_dwordx4 v[196:199], v224, s[64:67], s89 offen sc1\n\t"
    "s_waitcnt vmcnt(0)\n\t"
    "Lchk%=:\n\t"
    "v_xor_b32 v196, s56, v196\n\t"
    "v_xor_b32 v197, s56, v197\n\t"
    "v_xor_b32 v198, s56, v198\n\t"
    "v_xor_b32 v199, s56, v199\n\t"
    "v_or3_b32 v196, v196, v197, v198\n\t"
    "v_or_b32 v196, v196, v199\n\t"
    "v_cmp_ne_u32 vcc, 0, v196\n\t"
    "s_sub_u32 s57, s57, 1\n\t"
    "s_nop 1\n\t"
    "s_cbranch_vccz Lgot%=\n\t"
    "s_sleep 2\n\t"
    "s_cmp_lg_u32 s57, 0\n\t"
    "s_cbranch_scc1 Lpoll%=\n\t"
    "Lgot%=:\n\t"
    "s_lshr_b32 s51, %3, 3\n\t"
    "s_lshl_b32 s51, s51, 10\n\t"
    "buffer_load_dwordx4 a[240:243], v224, s[36:39], s51 offen offset:0 sc1\n\t"
    "v_mfma_f32_16x16x32_bf16 v[56:59], a[48:51], a[140:143], v[56:59]\n\t"
    "buffer_load_dwordx4 a[0:3], v224, s[36:39], s48 offen offset:0 sc1\n\t"
    "v_mul_f32 v238, v166, v38\n\t"
    "v_mul_f32 v239, v167, v39\n\t"
    "v_mfma_f32_16x16x32_bf16 v[56:59], a[52:55], a[156:159], v[56:59]\n\t"
    "s_waitcnt vmcnt(2) lgkmcnt(9)\n\t"
    "ds_read_b128 a[112:115], v251 offset:16960\n\t"
    "ds_read_b128 a[116:119], v252 offset:16960\n\t"
    "v_cvt_pk_bf16_f32 v234, v236, v237\n\t"
    "v_mfma_f32_16x16x32_bf16 v[56:59], a[56:59], a[172:175], v[56:59]\n\t"
    "ds_read_b128 a[120:123], v253 offset:16960\n\t"
    "ds_read_b128 a[124:127], v254 offset:16960\n\t"
    "v_cvt_pk_bf16_f32 v235, v238, v239\n\t"
    "v_mfma_f32_16x16x32_bf16 v[60:63], a[60:63], a[140:143], v[60:63]\n\t"
    "s_waitcnt lgkmcnt(8)\n\t"
    "buffer_load_dwordx4 a[4:7], v224, s[36:39], s48 offen offset:1024 sc1\n\t"
    "ds_write_b128 v224, v[128:131] offset:4096\n\t"
    "ds_read_b128 v[248:251], v230 offset:0\n\t"
    "ds_read_b128 v[252:255], v230 offset:16\n\t"
    "v_mfma_f32_16x16x32_bf16 v[60:63], a[64:67], a[156:159], v[60:63]\n\t"
    "ds_write_b128 v226, v[232:235] offset:64\n\t"
    "v_mfma_f32_16x16x32_bf16 v[60:63], a[68:71], a[172:175], v[60:63]\n\t"
    "v_mul_f32 v240, v168, v40\n\t"
    "v_mul_f32 v241, v169, v41\n\t"
    "v_mfma_f32_16x16x32_bf16 v[64:67], a[16:19], v[192:195], v[88:91]\n\t"
    "buffer_load_dwordx4 a[8:11], v224, s[36:39], s48 offen offset:2048 sc1\n\t"
    "v_mul_f32 v242, v170, v42\n\t"
    "v_mul_f32 v243, v171, v43\n\t"
    "v_mfma_f32_16x16x32_bf16 v[64:67], a[72:75], a[128:131], v[64:67]\n\t"
    "v_cvt_pk_bf16_f32 v240, v240, v241\n\t"
    "v_mul_f32 v244, v172, v44\n\t"
    "v_mfma_f32_16x16x32_bf16 v[64:67], a[76:79], a[144:147], v[64:67]\n\t"
    "v_mul_f32 v245, v173, v45\n\t"
    "v_cvt_pk_bf16_f32 v241, v242, v243\n\t"
    "v_mfma_f32_16x16x32_bf16 v[64:67], a[80:83], a[160:163], v[64:67]\n\t"
    "buffer_load_dwordx4 a[12:15], v224, s[36:39], s48 offen offset:3072 sc1\n\t"
    "s_add_u32 s48, s48, 4096\n\t"
    "v_mul_f32 v246, v174, v46\n\t"
    "v_mfma_f32_16x16x32_bf16 v[68:71], a[20:23], v[192:195], v[92:95]\n\t"
    "v_mul_f32 v247, v175, v47\n\t"
    "v_cvt_pk_bf16_f32 v242, v244, v245\n\t"
    "v_mfma_f32_16x16x32_bf16 v[68:71], a[84:87], a[128:131], v[68:71]\n\t"
    "v_cvt_pk_bf16_f32 v243, v246, v247\n\t"
    "v_mfma_f32_16x16x32_bf16 v[68:71], a[88:91], a[144:147], v[68:71]\n\t"
    "ds_write_b128 v226, v[240:243] offset:8384\n\t"
    "v_mfma_f32_16x16x32_bf16 v[68:71], a[92:95], a[160:163], v[68:71]\n\t"
    "s_waitcnt lgkmcnt(3)\n\t"
    "v_mul_f32 v232, v176, v48\n\t"
    "v_mul_f32 v233, v177, v49\n\t"
    "v_add_f32 v0, v248, v0\n\t"
    "v_mfma_f32_16x16x32_bf16 v[72:75], a[16:19], v[200:203], v[88:91]\n\t"
    "v_mul_f32 v234, v178, v50\n\t"
    "v_mul_f32 v235, v179, v51\n\t"
    "v_add_f32 v1, v249, v1\n\t"
    "v_mfma_f32_16x16x32_bf16 v[72:75], a[72:75], a[132:135], v[72:75]\n\t"
    "v_cvt_pk_bf16_f32 v232, v232, v233\n\t"
    "v_mul_f32 v236, v180, v52\n\t"
    "v_add_f32 v2, v250, v2\n\t"
    "v_mfma_f32_16x16x32_bf16 v[72:75], a[76:79], a[148:151], v[72:75]\n\t"
    "v_mul_f32 v237, v181, v53\n\t"
    "v_cvt_pk_bf16_f32 v233, v234, v235\n\t"
    "v_add_f32 v3, v251, v3\n\t"
    "v_mfma_f32_16x16x32_bf16 v[72:75], a[80:83], a[164:167], v[72:75]\n\t"
    "v_mul_f32 v238, v182, v54\n\t"
    "v_mul_f32 v239, v183, v55\n\t"
    "v_add_f32 v8, v248, v8\n\t"
    "v_mfma_f32_16x16x32_bf16 v[76:79], a[20:23], v[200:203], v[92:95]\n\t"
    "v_cvt_pk_bf16_f32 v234, v236, v237\n\t"
    "v_cvt_pk_bf16_f32 v235, v238, v239\n\t"
    "v_add_f32 v9, v249, v9\n\t"
    "v_mfma_f32_16x16x32_bf16 v[76:79], a[84:87], a[132:135], v[76:79]\n\t"
    "ds_write_b128 v226, v[232:235] offset:16704\n\t"
    "v_mfma_f32_16x16x32_bf16 v[76:79], a[88:91], a[148:151], v[76:79]\n\t"
    "v_mul_f32 v240, v184, v56\n\t"
    "v_mul_f32 v241, v185, v57\n\t"
    "v_add_f32 v10, v250, v10\n\t"
    "v_mfma_f32_16x16x32_bf16 v[76:79], a[92:95], a[164:167], v[76:79]\n\t"
    "v_mul_f32 v242, v186, v58\n\t"
    "v_mul_f32 v243, v187, v59\n\t"
    "v_mfma_f32_16x16x32_bf16 v[80:83], a[16:19], v[208:211], v[88:91]\n\t"
    "v_cvt_pk_bf16_f32 v240, v240, v241\n\t"
    "v_mul_f32 v244, v188, v60\n\t"
    "v_mfma_f32_16x16x32_bf16 v[80:83], a[72:75], a[136:139], v[80:83]\n\t"
    "v_mul_f32 v245, v189, v61\n\t"
    "v_cvt_pk_bf16_f32 v241, v242, v243\n\t"
    "v_mfma_f32_16x16x32_bf16 v[80:83], a[76:79], a[152:155], v[80:83]\n\t"
    "v_mul_f32 v246, v190, v62\n\t"
    "v_mul_f32 v247, v191, v63\n\t"
    "v_mfma_f32_16x16x32_bf16 v[80:83], a[80:83], a[168:171], v[80:83]\n\t"
    "v_cvt_pk_bf16_f32 v242, v244, v245\n\t"
    "v_cvt_pk_bf16_f32 v243, v246, v247\n\t"
    "v_mfma_f32_16x16x32_bf16 v[84:87], a[20:23], v[208:211], v[92:95]\n\t"
    "ds_write_b128 v226, v[240:243] offset:25024\n\t"
    "v_mfma_f32_16x16x32_bf16 v[84:87], a[84:87], a[136:139], v[84:87]\n\t"
    "ds_write_b128 v224, v[132:135] offset:5120\n\t"
    "v_mfma_f32_16x16x32_bf16 v[84:87], a[88:91], a[152:155], v[84:87]\n\t"
    "ds_read_b128 v[132:135], v226 offset:33280\n\t"
    "ds_write_b128 v224, v[136:139] offset:6144\n\t"
    "v_mfma_f32_16x16x32_bf16 v[84:87], a[92:95], a[168:171], v[84:87]\n\t"
    "ds_write_b128 v224, v[140:143] offset:7168\n\t"
    "v_mfma_f32_16x16x32_bf16 v[88:91], a[16:19], v[216:219], v[88:91]\n\t"
    "buffer_load_dwordx4 a[16:19], v224, s[36:39], s48 offen offset:0 sc1\n\t"
    "ds_read_b128 v[140:143], v226 offset:41600\n\t"
    "ds_write_b128 v224, v[144:147] offset:8192\n\t"
    "v_mfma_f32_16x16x32_bf16 v[88:91], a[72:75], a[140:143], v[88:91]\n\t"
    "ds_write_b128 v224, v[148:151] offset:9216\n\t"
    "v_mfma_f32_16x16x32_bf16 v[88:91], a[76:79], a[156:159], v[88:91]\n\t"
    "ds_read_b128 v[148:151], v226 offset:49920\n\t"
    "ds_write_b128 v224, v[152:155] offset:10240\n\t"
    "v_mfma_f32_16x16x32_bf16 v[88:91], a[80:83], a[172:175], v[88:91]\n\t"
    "ds_write_b128 v224, v[156:159] offset:11264\n\t"
    "v_mfma_f32_16x16x32_bf16 v[92:95], a[20:23], v[216:219], v[92:95]\n\t"
    "s_waitcnt lgkmcnt(10)\n\t"
    "buffer_load_dwordx4 a[20:23], v224, s[36:39], s48 offen offset:1024 sc1\n\t"
    "ds_read_b128 v[156:159], v226 offset:58240\n\t"
    "ds_write_b128 v224, v[160:163] offset:12288\n\t"
    "v_mfma_f32_16x16x32_bf16 v[92:95], a[84:87], a[140:143], v[92:95]\n\t"
    "ds_write_b128 v224, v[164:167] offset:13312\n\t"
    "v_mfma_f32_16x16x32_bf16 v[92:95], a[88:91], a[156:159], v[92:95]\n\t"
    "ds_read_b128 v[164:167], v226 offset:33344\n\t"
    "v_mfma_f32_16x16x32_bf16 v[92:95], a[92:95], a[172:175], v[92:95]\n\t"
    "s_waitcnt lgkmcnt(12)\n\t"
    "buffer_load_dwordx4 a[24:27], v224, s[36:39], s48 offen offset:2048 sc1\n\t"
    "v_lshlrev_b32 v128, 16, v132\n\t"
    "v_and_b32 v129, s50, v132\n\t"
    "v_lshlrev_b32 v130, 16, v133\n\t"
    "v_mfma_f32_16x16x32_bf16 v[96:99], a[96:99], v[192:195], v[120:123]\n\t"
    "v_and_b32 v131, s50, v133\n\t"
    "v_lshlrev_b32 v132, 16, v134\n\t"
    "v_and_b32 v133, s50, v134\n\t"
    "v_mfma_f32_16x16x32_bf16 v[96:99], a[100:103], a[128:131], v[96:99]\n\t"
    "s_waitcnt lgkmcnt(9)\n\t"
    "v_lshlrev_b32 v134, 16, v135\n\t"
    "v_and_b32 v135, s50, v135\n\t"
    "v_lshlrev_b32 v136, 16, v140\n\t"
    "v_mfma_f32_16x16x32_bf16 v[96:99], a[104:107], a[144:147], v[96:99]\n\t"
    "buffer_load_dwordx4 a[28:31], v224, s[36:39], s48 offen offset:3072 sc1\n\t"
    "s_add_u32 s48, s48, 4096\n\t"
    "v_and_b32 v137, s50, v140\n\t"
    "v_lshlrev_b32 v138, 16, v141\n\t"
    "v_and_b32 v139, s50, v141\n\t"
    "v_mfma_f32_16x16x32_bf16 v[96:99], a[108:111], a[160:163], v[96:99]\n\t"
    "v_lshlrev_b32 v140, 16, v142\n\t"
    "v_and_b32 v141, s50, v142\n\t"
    "v_lshlrev_b32 v142, 16, v143\n\t"
    "v_mfma_f32_16x16x32_bf16 v[100:103], a[112:115], v[192:195], v[124:127]\n\t"
    "s_waitcnt lgkmcnt(6)\n\t"
    "v_and_b32 v143, s50, v143\n\t"
    "v_lshlrev_b32 v144, 16, v148\n\t"
    "v_and_b32 v145, s50, v148\n\t"
    "v_mfma_f32_16x16x32_bf16 v[100:103], a[116:119], a[128:131], v[100:103]\n\t"
    "buffer_load_dwordx4 a[32:35], v224, s[36:39], s48 offen offset:0 sc1\n\t"
    "v_lshlrev_b32 v146, 16, v149\n\t"
    "v_and_b32 v147, s50, v149\n\t"
    "v_lshlrev_b32 v148, 16, v150\n\t"
    "v_mfma_f32_16x16x32_bf16 v[100:103], a[120:123], a[144:147], v[100:103]\n\t"
    "v_and_b32 v149, s50, v150\n\t"
    "v_lshlrev_b32 v150, 16, v151\n\t"
    "v_and_b32 v151, s50, v151\n\t"
    "v_mfma_f32_16x16x32_bf16 v[100:103], a[124:127], a[160:163], v[100:103]\n\t"
    "s_waitcnt lgkmcnt(3)\n\t"
    "v_lshlrev_b32 v152, 16, v156\n\t"
    "v_and_b32 v153, s50, v156\n\t"
    "v_lshlrev_b32 v154, 16, v157\n\t"
    "v_mfma_f32_16x16x32_bf16 v[104:107], a[96:99], v[200:203], v[120:123]\n\t"
    "buffer_load_dwordx4 a[36:39], v224, s[36:39], s48 offen offset:1024 sc1\n\t"
    "v_and_b32 v155, s50, v157\n\t"
    "v_lshlrev_b32 v156, 16, v158\n\t"
    "v_and_b32 v157, s50, v158\n\t"
    "v_mfma_f32_16x16x32_bf16 v[104:107], a[100:103], a[132:135], v[104:107]\n\t"
    "s_waitcnt lgkmcnt(0)\n\t"
    "v_lshlrev_b32 v158, 16, v159\n\t"
    "v_and_b32 v159, s50, v159\n\t"
    "v_lshlrev_b32 v160, 16, v164\n\t"
    "v_mfma_f32_16x16x32_bf16 v[104:107], a[104:107], a[148:151], v[104:107]\n\t"
    "v_and_b32 v161, s50, v164\n\t"
    "v_lshlrev_b32 v162, 16, v165\n\t"
    "v_and_b32 v163, s50, v165\n\t"
    "v_mfma_f32_16x16x32_bf16 v[104:107], a[108:111], a[164:167], v[104:107]\n\t"
    "buffer_load_dwordx4 a[40:43], v224, s[36:39], s48 offen offset:2048 sc1\n\t"
    "v_lshlrev_b32 v164, 16, v166\n\t"
    "v_and_b32 v165, s50, v166\n\t"
    "v_lshlrev_b32 v166, 16, v167\n\t"
    "v_mfma_f32_16x16x32_bf16 v[108:111], a[112:115], v[200:203], v[124:127]\n\t"
    "v_and_b32 v167, s50, v167\n\t"
    "v_mul_f32 v232, v128, v64\n\t"
    "v_mul_f32 v233, v129, v65\n\t"
    "v_mfma_f32_16x16x32_bf16 v[108:111], a[116:119], a[132:135], v[108:111]\n\t"
    "ds_write_b128 v224, v[168:171] offset:14336\n\t"
    "v_mfma_f32_16x16x32_bf16 v[108:111], a[120:123], a[148:151], v[108:111]\n\t"
    "buffer_load_dwordx4 a[44:47], v224, s[36:39], s48 offen offset:3072 sc1\n\t"
    "s_add_u32 s48, s48, 4096\n\t"
    "ds_write_b128 v224, v[172:175] offset:15360\n\t"
    "v_mfma_f32_16x16x32_bf16 v[108:111], a[124:127], a[164:167], v[108:111]\n\t"
    "ds_read_b128 v[172:175], v226 offset:41664\n\t"
    "ds_write_b128 v224, v[176:179] offset:16384\n\t"
    "v_mfma_f32_16x16x32_bf16 v[112:115], a[96:99], v[208:211], v[120:123]\n\t"
    "ds_write_b128 v224, v[180:183] offset:17408\n\t"
    "v_mfma_f32_16x16x32_bf16 v[112:115], a[100:103], a[136:139], v[112:115]\n\t"
    "buffer_load_dwordx4 a[48:51], v224, s[36:39], s48 offen offset:0 sc1\n\t"
    "ds_read_b128 v[180:183], v226 offset:49984\n\t"
    "ds_write_b128 v224, v[184:187] offset:18432\n\t"
    "v_mfma_f32_16x16x32_bf16 v[112:115], a[104:107], a[152:155], v[112:115]\n\t"
    "ds_write_b128 v224, v[188:191] offset:19456\n\t"
    "v_mfma_f32_16x16x32_bf16 v[112:115], a[108:111], a[168:171], v[112:115]\n\t"
    "s_waitcnt lgkmcnt(0)\n\t"
    "s_barrier\n\t"
    "v_mfma_f32_16x16x32_bf16 v[116:119], a[112:115], v[208:211], v[124:127]\n\t"
    "ds_read_b128 a[176:179], v225 offset:0\n\t"
    "buffer_load_dwordx4 a[52:55], v224, s[36:39], s48 offen offset:1024 sc1\n\t"
    "ds_read_b128 v[188:191], v226 offset:58304\n\t"
    "v_mul_f32 v234, v130, v66\n\t"
    "v_mul_f32 v235, v131, v67\n\t"
    "v_cvt_pk_bf16_f32 v232, v232, v233\n\t"
    "v_mfma_f32_16x16x32_bf16 v[116:119], a[116:119], a[136:139], v[116:119]\n\t"
    "ds_read_b128 a[180:183], v225 offset:8320\n\t"
    "v_mul_f32 v236, v132, v68\n\t"
    "v_mul_f32 v237, v133, v69\n\t"
    "v_cvt_pk_bf16_f32 v233, v234, v235\n\t"
    "v_mfma_f32_16x16x32_bf16 v[116:119], a[120:123], a[152:155], v[116:119]\n\t"
    "v_mul_f32 v238, v134, v70\n\t"
    "v_mul_f32 v239, v135, v71\n\t"
    "v_cvt_pk_bf16_f32 v234, v236, v237\n\t"
    "v_mfma_f32_16x16x32_bf16 v[116:119], a[124:127], a[168:171], v[116:119]\n\t"
    "buffer_load_dwordx4 a[56:59], v224, s[36:39], s48 offen offset:2048 sc1\n\t"
    "v_cvt_pk_bf16_f32 v235, v238, v239\n\t"
    "v_mfma_f32_16x16x32_bf16 v[120:123], a[96:99], v[216:219], v[120:123]\n\t"
    "ds_read_b128 a[184:187], v225 offset:16640\n\t"
    "v_lshlrev_b32 v168, 16, v172\n\t"
    "v_and_b32 v169, s50, v172\n\t"
    "v_lshlrev_b32 v170, 16, v173\n\t"
    "v_mfma_f32_16x16x32_bf16 v[120:123], a[100:103], a[140:143], v[120:123]\n\t"
    "ds_read_b128 a[188:191], v225 offset:24960\n\t"
    "v_and_b32 v171, s50, v173\n\t"
    "v_lshlrev_b32 v172, 16, v174\n\t"
    "v_and_b32 v173, s50, v174\n\t"
    "v_mfma_f32_16x16x32_bf16 v[120:123], a[104:107], a[156:159], v[120:123]\n\t"
    "buffer_load_dwordx4 a[60:63], v224, s[36:39], s48 offen offset:3072 sc1\n\t"
    "s_add_u32 s48, s48, 4096\n\t"
    "v_lshlrev_b32 v174, 16, v175\n\t"
    "v_and_b32 v175, s50, v175\n\t"
    "v_lshlrev_b32 v176, 16, v180\n\t"
    "v_mfma_f32_16x16x32_bf16 v[120:123], a[108:111], a[172:175], v[120:123]\n\t"
    "v_and_b32 v177, s50, v180\n\t"
    "v_lshlrev_b32 v178, 16, v181\n\t"
    "v_and_b32 v179, s50, v181\n\t"
    "v_mfma_f32_16x16x32_bf16 v[124:127], a[112:115], v[216:219], v[124:127]\n\t"
    "ds_read_b128 a[192:195], v225 offset:16\n\t"
    "v_lshlrev_b32 v180, 16, v182\n\t"
    "v_and_b32 v181, s50, v182\n\t"
    "v_lshlrev_b32 v182, 16, v183\n\t"
    "v_mfma_f32_16x16x32_bf16 v[124:127], a[116:119], a[140:143], v[124:127]\n\t"
    "s_waitcnt lgkmcnt(4)\n\t"
    "ds_read_b128 a[196:199], v225 offset:8336\n\t"
    "buffer_load_dwordx4 a[64:67], v224, s[36:39], s48 offen offset:0 sc1\n\t"
    "v_and_b32 v183, s50, v183\n\t"
    "v_lshlrev_b32 v184, 16, v188\n\t"
    "v_and_b32 v185, s50, v188\n\t"
    "v_mfma_f32_16x16x32_bf16 v[124:127], a[120:123], a[156:159], v[124:127]\n\t"
    "v_lshlrev_b32 v186, 16, v189\n\t"
    "v_and_b32 v187, s50, v189\n\t"
    "v_lshlrev_b32 v188, 16, v190\n\t"
    "v_mfma_f32_16x16x32_bf16 v[124:127], a[124:127], a[172:175], v[124:127]\n\t"
    "v_and_b32 v189, s50, v190\n\t"
    "v_lshlrev_b32 v190, 16, v191\n\t"
    "v_and_b32 v191, s50, v191\n\t"
    "buffer_load_dwordx4 a[68:71], v224, s[36:39], s48 offen offset:1024 sc1\n\t"
    "ds_write_b128 v226, v[232:235] offset:33280\n\t"
    "v_mul_f32 v240, v136, v72\n\t"
    "v_mul_f32 v241, v137, v73\n\t"
    "v_mul_f32 v242, v138, v74\n\t"
    "v_mul_f32 v243, v139, v75\n\t"
    "v_cvt_pk_bf16_f32 v240, v240, v241\n\t"
    "v_mul_f32 v244, v140, v76\n\t"
    "buffer_load_dwordx4 a[72:75], v224, s[36:39], s48 offen offset:2048 sc1\n\t"
    "v_mul_f32 v245, v141, v77\n\t"
    "v_cvt_pk_bf16_f32 v241, v242, v243\n\t"
    "v_mul_f32 v246, v142, v78\n\t"
    "v_mul_f32 v247, v143, v79\n\t"
    "v_cvt_pk_bf16_f32 v242, v244, v245\n\t"
    "v_cvt_pk_bf16_f32 v243, v246, v247\n\t"
    "ds_write_b128 v226, v[240:243] offset:41600\n\t"
    "buffer_load_dwordx4 a[76:79], v224, s[36:39], s48 offen offset:3072 sc1\n\t"
    "s_add_u32 s48, s48, 4096\n\t"
    "v_mul_f32 v232, v144, v80\n\t"
    "v_mul_f32 v233, v145, v81\n\t"
    "v_mul_f32 v234, v146, v82\n\t"
    "v_mul_f32 v235, v147, v83\n\t"
    "v_cvt_pk_bf16_f32 v232, v232, v233\n\t"
    "v_mul_f32 v236, v148, v84\n\t"
    "v_mul_f32 v237, v149, v85\n\t"
    "v_cvt_pk_bf16_f32 v233, v234, v235\n\t"
    "v_mul_f32 v238, v150, v86\n\t"
    "buffer_load_dwordx4 a[80:83], v224, s[36:39], s48 offen offset:0 sc1\n\t"
    "v_mul_f32 v239, v151, v87\n\t"
    "v_cvt_pk_bf16_f32 v234, v236, v237\n\t"
    "v_cvt_pk_bf16_f32 v235, v238, v239\n\t"
    "ds_write_b128 v226, v[232:235] offset:49920\n\t"
    "v_mul_f32 v240, v152, v88\n\t"
    "v_mul_f32 v241, v153, v89\n\t"
    "v_mul_f32 v242, v154, v90\n\t"
    "buffer_load_dwordx4 a[84:87], v224, s[36:39], s48 offen offset:1024 sc1\n\t"
    "v_mul_f32 v243, v155, v91\n\t"
    "v_cvt_pk_bf16_f32 v240, v240, v241\n\t"
    "buffer_load_dwordx4 a[88:91], v224, s[36:39], s48 offen offset:2048 sc1\n\t"
    "buffer_load_dwordx4 a[92:95], v224, s[36:39], s48 offen offset:3072 sc1\n\t"
    "s_add_u32 s48, s48, 4096\n\t"
    "buffer_load_dwordx4 a[96:99], v224, s[36:39], s48 offen offset:0 sc1\n\t"
    "buffer_load_dwordx4 a[100:103], v224, s[36:39], s48 offen offset:1024 sc1\n\t"
    "s_sub_u32 s49, s49, 1\n\t"
    "s_branch Lloop%=\n\t"
    "Llast%=:\n\t"
    "v_subrev_u32 v248, s58, v224\n\t"
    "s_mul_i32 s51, %1, 8320\n\t"
    "s_add_u32 s51, s51, %2\n\t"
    "v_and_b32 v251, 0x30, v248\n\t"
    "v_bfe_u32 v250, v248, 6, 3\n\t"
    "v_lshl_add_u32 v249, v250, 7, v251\n\t"
    "v_bfe_u32 v250, v248, 6, 2\n\t"
    "v_lshl_add_u32 v251, v250, 8, v251\n\t"
    "v_bfe_u32 v250, v248, 8, 1\n\t"
    "v_lshl_add_u32 v251, v250, 13, v251\n\t"
    "v_lshrrev_b32 v250, 9, v248\n\t"
    "v_lshl_add_u32 v251, v250, 10, v251\n\t"
    "v_mul_u32_u24 v250, 1040, v250\n\t"
    "v_add3_u32 v249, v249, v250, s51\n\t"
    "s_lshl_b32 s94, %1, 14\n\t"
    "s_add_u32 s95, s94, 4096\n\t"
    "v_mfma_f32_16x16x32_bf16 v[88:91], a[16:19], v[216:219], v[88:91]\n\t"
    "v_mfma_f32_16x16x32_bf16 v[92:95], a[20:23], v[216:219], v[92:95]\n\t"
    "v_mfma_f32_16x16x32_bf16 v[0:3], a[24:27], a[128:131], v[0:3]\n\t"
    "v_mfma_f32_16x16x32_bf16 v[0:3], a[28:31], a[144:147], v[0:3]\n\t"
    "s_waitcnt vmcnt(20)\n\t"
    "v_mfma_f32_16x16x32_bf16 v[0:3], a[32:35], a[160:163], v[0:3]\n\t"
    "v_mfma_f32_16x16x32_bf16 v[4:7], a[36:39], a[128:131], v[4:7]\n\t"
    "v_mfma_f32_16x16x32_bf16 v[4:7], a[40:43], a[144:147], v[4:7]\n\t"
    "v_mfma_f32_16x16x32_bf16 v[4:7], a[44:47], a[160:163], v[4:7]\n\t"
    "v_mfma_f32_16x16x32_bf16 v[8:11], a[24:27], a[132:135], v[8:11]\n\t"
    "v_mfma_f32_16x16x32_bf16 v[8:11], a[28:31], a[148:151], v[8:11]\n\t"
    "s_waitcnt lgkmcnt(10)\n\t"
    "v_mfma_f32_16x16x32_bf16 v[8:11], a[32:35], a[164:167], v[8:11]\n\t"
    "s_waitcnt lgkmcnt(2)\n\t"
    "v_mul_f32 v232, v196, v0\n\t"
    "v_mul_f32 v233, v197, v1\n\t"
    "v_mfma_f32_16x16x32_bf16 v[12:15], a[36:39], a[132:135], v[12:15]\n\t"
    "v_mul_f32 v234, v198, v2\n\t"
    "v_mul_f32 v235, v199, v3\n\t"
    "v_mfma_f32_16x16x32_bf16 v[12:15], a[40:43], a[148:151], v[12:15]\n\t"
    "v_cvt_pk_bf16_f32 v232, v232, v233\n\t"
    "v_mul_f32 v236, v204, v4\n\t"
    "v_mfma_f32_16x16x32_bf16 v[12:15], a[44:47], a[164:167], v[12:15]\n\t"
    "v_mul_f32 v237, v205, v5\n\t"
    "v_cvt_pk_bf16_f32 v233, v234, v235\n\t"
    "v_mfma_f32_16x16x32_bf16 v[16:19], a[24:27], a[136:139], v[16:19]\n\t"
    "v_mul_f32 v238, v206, v6\n\t"
    "v_mul_f32 v239, v207, v7\n\t"
    "v_mfma_f32_16x16x32_bf16 v[16:19], a[28:31], a[152:155], v[16:19]\n\t"
    "ds_read_b128 v[196:199], v224 offset:8192\n\t"
    "ds_read_b128 v[204:207], v224 offset:9216\n\t"
    "v_cvt_pk_bf16_f32 v234, v236, v237\n\t"
    "v_mfma_f32_16x16x32_bf16 v[16:19], a[32:35], a[168:171], v[16:19]\n\t"
    "v_cvt_pk_bf16_f32 v235, v238, v239\n\t"
    "v_mfma_f32_16x16x32_bf16 v[20:23], a[36:39], a[136:139], v[20:23]\n\t"
    "ds_write_b128 v226, v[232:235] offset:0\n\t"
    "v_mfma_f32_16x16x32_bf16 v[20:23], a[40:43], a[152:155], v[20:23]\n\t"
    "s_waitcnt lgkmcnt(3)\n\t"
    "v_mul_f32 v240, v212, v8\n\t"
    "v_mul_f32 v241, v213, v9\n\t"
    "v_mfma_f32_16x16x32_bf16 v[20:23], a[44:47], a[168:171], v[20:23]\n\t"
    "v_mul_f32 v242, v214, v10\n\t"
    "v_mul_f32 v243, v215, v11\n\t"
    "v_mfma_f32_16x16x32_bf16 v[24:27], a[24:27], a[140:143], v[24:27]\n\t"
    "v_cvt_pk_bf16_f32 v240, v240, v241\n\t"
    "v_mul_f32 v244, v220, v12\n\t"
    "v_mfma_f32_16x16x32_bf16 v[24:27], a[28:31], a[156:159], v[24:27]\n\t"
    "v_mul_f32 v245, v221, v13\n\t"
    "v_cvt_pk_bf16_f32 v241, v242, v243\n\t"
    "v_mfma_f32_16x16x32_bf16 v[24:27], a[32:35], a[172:175], v[24:27]\n\t"
    "v_mul_f32 v246, v222, v14\n\t"
    "v_mul_f32 v247, v223, v15\n\t"
    "v_mfma_f32_16x16x32_bf16 v[28:31], a[36:39], a[140:143], v[28:31]\n\t"
    "ds_read_b128 v[212:215], v224 offset:10240\n\t"
    "ds_read_b128 v[220:223], v224 offset:11264\n\t"
    "v_cvt_pk_bf16_f32 v242, v244, v245\n\t"
    "v_mfma_f32_16x16x32_bf16 v[28:31], a[40:43], a[156:159], v[28:31]\n\t"
    "v_cvt_pk_bf16_f32 v243, v246, v247\n\t"
    "v_mfma_f32_16x16x32_bf16 v[28:31], a[44:47], a[172:175], v[28:31]\n\t"
    "s_waitcnt vmcnt(16)\n\t"
    "ds_write_b128 v226, v[240:243] offset:8320\n\t"
    "v_mfma_f32_16x16x32_bf16 v[32:35], a[48:51], a[128:131], v[32:35]\n\t"
    "s_waitcnt lgkmcnt(4)\n\t"
    "v_mul_f32 v232, v196, v16\n\t"
    "v_mul_f32 v233, v197, v17\n\t"
    "v_mfma_f32_16x16x32_bf16 v[32:35], a[52:55], a[144:147], v[32:35]\n\t"
    "v_mul_f32 v234, v198, v18\n\t"
    "v_mul_f32 v235, v199, v19\n\t"
    "v_mfma_f32_16x16x32_bf16 v[32:35], a[56:59], a[160:163], v[32:35]\n\t"
    "v_cvt_pk_bf16_f32 v232, v232, v233\n\t"
    "v_mul_f32 v236, v204, v20\n\t"
    "v_mfma_f32_16x16x32_bf16 v[36:39], a[60:63], a[128:131], v[36:39]\n\t"
    "s_waitcnt vmcnt(12)\n\t"
    "v_mul_f32 v237, v205, v21\n\t"
    "v_cvt_pk_bf16_f32 v233, v234, v235\n\t"
    "v_mfma_f32_16x16x32_bf16 v[36:39], a[64:67], a[144:147], v[36:39]\n\t"
    "v_mul_f32 v238, v206, v22\n\t"
    "v_mul_f32 v239, v207, v23\n\t"
    "v_mfma_f32_16x16x32_bf16 v[36:39], a[68:71], a[160:163], v[36:39]\n\t"
    "ds_read_b128 v[196:199], v224 offset:12288\n\t"
    "ds_read_b128 v[204:207], v224 offset:13312\n\t"
    "v_cvt_pk_bf16_f32 v234, v236, v237\n\t"
    "v_mfma_f32_16x16x32_bf16 v[40:43], a[48:51], a[132:135], v[40:43]\n\t"
    "v_cvt_pk_bf16_f32 v235, v238, v239\n\t"
    "v_mfma_f32_16x16x32_bf16 v[40:43], a[52:55], a[148:151], v[40:43]\n\t"
    "ds_write_b128 v226, v[232:235] offset:16640\n\t"
    "v_mfma_f32_16x16x32_bf16 v[40:43], a[56:59], a[164:167], v[40:43]\n\t"
    "s_waitcnt lgkmcnt(4)\n\t"
    "v_mul_f32 v240, v212, v24\n\t"
    "v_mul_f32 v241, v213, v25\n\t"
    "v_mfma_f32_16x16x32_bf16 v[44:47], a[60:63], a[132:135], v[44:47]\n\t"
    "v_mul_f32 v242, v214, v26\n\t"
    "v_mul_f32 v243, v215, v27\n\t"
    "v_mfma_f32_16x16x32_bf16 v[44:47], a[64:67], a[148:151], v[44:47]\n\t"
    "v_cvt_pk_bf16_f32 v240, v240, v241\n\t"
    "v_mul_f32 v244, v220, v28\n\t"
    "v_mfma_f32_16x16x32_bf16 v[44:47], a[68:71], a[164:167], v[44:47]\n\t"
    "v_mul_f32 v245, v221, v29\n\t"
    "v_cvt_pk_bf16_f32 v241, v242, v243\n\t"
    "v_mfma_f32_16x16x32_bf16 v[48:51], a[48:51], a[136:139], v[48:51]\n\t"
    "v_mul_f32 v246, v222, v30\n\t"
    "v_mul_f32 v247, v223, v31\n\t"
    "v_mfma_f32_16x16x32_bf16 v[48:51], a[52:55], a[152:155], v[48:51]\n\t"
    "ds_read_b128 v[212:215], v224 offset:14336\n\t"
    "ds_read_b128 v[220:223], v224 offset:15360\n\t"
    "v_cvt_pk_bf16_f32 v242, v244, v245\n\t"
    "v_mfma_f32_16x16x32_bf16 v[48:51], a[56:59], a[168:171], v[48:51]\n\t"
    "v_cvt_pk_bf16_f32 v243, v246, v247\n\t"
    "v_mfma_f32_16x16x32_bf16 v[52:55], a[60:63], a[136:139], v[52:55]\n\t"
    "ds_write_b128 v226, v[240:243] offset:24960\n\t"
    "v_mfma_f32_16x16x32_bf16 v[52:55], a[64:67], a[152:155], v[52:55]\n\t"
    "s_waitcnt lgkmcnt(4)\n\t"
    "v_mul_f32 v232, v196, v32\n\t"
    "v_mul_f32 v233, v197, v33\n\t"
    "v_mfma_f32_16x16x32_bf16 v[52:55], a[68:71], a[168:171], v[52:55]\n\t"
    "s_waitcnt lgkmcnt(0)\n\t"
    "s_barrier\n\t"
    "v_mfma_f32_16x16x32_bf16 v[56:59], a[48:51], a[140:143], v[56:59]\n\t"
    "ds_read_b128 a[176:179], v249 offset:0\n\t"
    "ds_read_b128 a[180:183], v249 offset:2080\n\t"
    "v_mul_f32 v234, v198, v34\n\t"
    "v_mfma_f32_16x16x32_bf16 v[56:59], a[52:55], a[156:159], v[56:59]\n\t"
    "ds_read_b128 a[184:187], v249 offset:4160\n\t"
    "ds_read_b128 a[188:191], v249 offset:6240\n\t"
    "v_mul_f32 v235, v199, v35\n\t"
    "v_mfma_f32_16x16x32_bf16 v[56:59], a[56:59], a[172:175], v[56:59]\n\t"
    "v_cvt_pk_bf16_f32 v232, v232, v233\n\t"
    "v_mul_f32 v236, v204, v36\n\t"
    "v_mfma_f32_16x16x32_bf16 v[60:63], a[60:63], a[140:143], v[60:63]\n\t"
    "v_mul_f32 v237, v205, v37\n\t"
    "v_cvt_pk_bf16_f32 v233, v234, v235\n\t"
    "v_mfma_f32_16x16x32_bf16 v[60:63], a[64:67], a[156:159], v[60:63]\n\t"
    "v_mul_f32 v238, v206, v38\n\t"
    "v_mul_f32 v239, v207, v39\n\t"
    "v_mfma_f32_16x16x32_bf16 v[60:63], a[68:71], a[172:175], v[60:63]\n\t"
    "ds_read_b128 v[196:199], v224 offset:16384\n\t"
    "ds_read_b128 v[204:207], v224 offset:17408\n\t"
    "v_cvt_pk_bf16_f32 v234, v236, v237\n\t"
    "v_mfma_f32_16x16x32_bf16 v[64:67], a[72:75], a[128:131], v[64:67]\n\t"
    "v_cvt_pk_bf16_f32 v235, v238, v239\n\t"
    "v_mfma_f32_16x16x32_bf16 v[64:67], a[76:79], a[144:147], v[64:67]\n\t"
    "s_waitcnt vmcnt(8)\n\t"
    "ds_write_b128 v226, v[232:235] offset:64\n\t"
    "v_mfma_f32_16x16x32_bf16 v[64:67], a[80:83], a[160:163], v[64:67]\n\t"
    "v_mul_f32 v240, v212, v40\n\t"
    "v_mul_f32 v241, v213, v41\n\t"
    "v_mfma_f32_16x16x32_bf16 v[68:71], a[84:87], a[128:131], v[68:71]\n\t"
    "s_waitcnt lgkmcnt(5)\n\t"
    "buffer_store_dwordx4 a[176:179], v251, s[44:47], s94 offen offset:0 sc1\n\t"
    "v_mul_f32 v242, v214, v42\n\t"
    "v_mfma_f32_16x16x32_bf16 v[68:71], a[88:91], a[144:147], v[68:71]\n\t"
    "v_mul_f32 v243, v215, v43\n\t"
    "v_cvt_pk_bf16_f32 v240, v240, v241\n\t"
    "v_mfma_f32_16x16x32_bf16 v[68:71], a[92:95], a[160:163], v[68:71]\n\t"
    "buffer_store_dwordx4 a[180:183], v251, s[44:47], s94 offen offset:2048 sc1\n\t"
    "v_mul_f32 v244, v220, v44\n\t"
    "v_mul_f32 v245, v221, v45\n\t"
    "v_mfma_f32_16x16x32_bf16 v[72:75], a[72:75], a[132:135], v[72:75]\n\t"
    "v_cvt_pk_bf16_f32 v241, v242, v243\n\t"
    "v_mul_f32 v246, v222, v46\n\t"
    "v_mfma_f32_16x16x32_bf16 v[72:75], a[76:79], a[148:151], v[72:75]\n\t"
    "s_waitcnt lgkmcnt(3)\n\t"
    "buffer_store_dwordx4 a[184:187], v251, s[44:47], s95 offen offset:0 sc1\n\t"
    "v_mul_f32 v247, v223, v47\n\t"
    "v_mfma_f32_16x16x32_bf16 v[72:75], a[80:83], a[164:167], v[72:75]\n\t"
    "ds_read_b128 v[212:215], v224 offset:18432\n\t"
    "ds_read_b128 v[220:223], v224 offset:19456\n\t"
    "v_cvt_pk_bf16_f32 v242, v244, v245\n\t"
    "v_mfma_f32_16x16x32_bf16 v[76:79], a[84:87], a[132:135], v[76:79]\n\t"
    "buffer_store_dwordx4 a[188:191], v251, s[44:47], s95 offen offset:2048 sc1\n\t"
    "v_cvt_pk_bf16_f32 v243, v246, v247\n\t"
    "v_mfma_f32_16x16x32_bf16 v[76:79], a[88:91], a[148:151], v[76:79]\n\t"
    "ds_write_b128 v226, v[240:243] offset:8384\n\t"
    "v_mfma_f32_16x16x32_bf16 v[76:79], a[92:95], a[164:167], v[76:79]\n\t"
    "s_waitcnt lgkmcnt(4)\n\t"
    "v_mul_f32 v232, v196, v48\n\t"
    "v_mul_f32 v233, v197, v49\n\t"
    "v_mfma_f32_16x16x32_bf16 v[80:83], a[72:75], a[136:139], v[80:83]\n\t"
    "v_mul_f32 v234, v198, v50\n\t"
    "v_mul_f32 v235, v199, v51\n\t"
    "v_mfma_f32_16x16x32_bf16 v[80:83], a[76:79], a[152:155], v[80:83]\n\t"
    "v_cvt_pk_bf16_f32 v232, v232, v233\n\t"
    "v_mul_f32 v236, v204, v52\n\t"
    "v_mfma_f32_16x16x32_bf16 v[80:83], a[80:83], a[168:171], v[80:83]\n\t"
    "v_mul_f32 v237, v205, v53\n\t"
    "v_cvt_pk_bf16_f32 v233, v234, v235\n\t"
    "v_mfma_f32_16x16x32_bf16 v[84:87], a[84:87], a[136:139], v[84:87]\n\t"
    "v_mul_f32 v238, v206, v54\n\t"
    "v_mul_f32 v239, v207, v55\n\t"
    "v_mfma_f32_16x16x32_bf16 v[84:87], a[88:91], a[152:155], v[84:87]\n\t"
    "v_cvt_pk_bf16_f32 v234, v236, v237\n\t"
    "v_cvt_pk_bf16_f32 v235, v238, v239\n\t"
    "v_mfma_f32_16x16x32_bf16 v[84:87], a[92:95], a[168:171], v[84:87]\n\t"
    "ds_write_b128 v226, v[232:235] offset:16704\n\t"
    "v_mfma_f32_16x16x32_bf16 v[88:91], a[72:75], a[140:143], v[88:91]\n\t"
    "s_waitcnt lgkmcnt(2)\n\t"
    "v_mul_f32 v240, v212, v56\n\t"
    "v_mul_f32 v241, v213, v57\n\t"
    "v_mfma_f32_16x16x32_bf16 v[88:91], a[76:79], a[156:159], v[88:91]\n\t"
    "v_mul_f32 v242, v214, v58\n\t"
    "v_mul_f32 v243, v215, v59\n\t"
    "v_mfma_f32_16x16x32_bf16 v[88:91], a[80:83], a[172:175], v[88:91]\n\t"
    "v_cvt_pk_bf16_f32 v240, v240, v241\n\t"
    "v_mul_f32 v244, v220, v60\n\t"
    "v_mfma_f32_16x16x32_bf16 v[92:95], a[84:87], a[140:143], v[92:95]\n\t"
    "v_mul_f32 v245, v221, v61\n\t"
    "v_cvt_pk_bf16_f32 v241, v242, v243\n\t"
    "v_mfma_f32_16x16x32_bf16 v[92:95], a[88:91], a[156:159], v[92:95]\n\t"
    "v_mul_f32 v246, v222, v62\n\t"
    "v_mul_f32 v247, v223, v63\n\t"
    "v_mfma_f32_16x16x32_bf16 v[92:95], a[92:95], a[172:175], v[92:95]\n\t"
    "s_waitcnt vmcnt(8)\n\t"
    "v_cvt_pk_bf16_f32 v242, v244, v245\n\t"
    "v_cvt_pk_bf16_f32 v243, v246, v247\n\t"
    "v_mfma_f32_16x16x32_bf16 v[96:99], a[96:99], v[192:195], v[96:99]\n\t"
    "ds_write_b128 v226, v[240:243] offset:25024\n\t"
    "v_mfma_f32_16x16x32_bf16 v[96:99], a[100:103], a[128:131], v[96:99]\n\t"
    "v_mul_f32 v232, v128, v64\n\t"
    "v_mul_f32 v233, v129, v65\n\t"
    "v_mfma_f32_16x16x32_bf16 v[96:99], a[104:107], a[144:147], v[96:99]\n\t"
    "s_waitcnt lgkmcnt(0)\n\t"
    "s_barrier\n\t"
    "v_mfma_f32_16x16x32_bf16 v[96:99], a[108:111], a[160:163], v[96:99]\n\t"
    "s_waitcnt vmcnt(4)\n\t"
    "ds_read_b128 a[192:195], v249 offset:64\n\t"
    "v_mul_f32 v234, v130, v66\n\t"
    "v_mfma_f32_16x16x32_bf16 v[100:103], a[112:115], v[192:195], v[100:103]\n\t"
    "ds_read_b128 a[196:199], v249 offset:2144\n\t"
    "ds_read_b128 a[200:203], v249 offset:4224\n\t"
    "v_mul_f32 v235, v131, v67\n\t"
    "v_mfma_f32_16x16x32_bf16 v[100:103], a[116:119], a[128:131], v[100:103]\n\t"
    "ds_read_b128 a[204:207], v249 offset:6304\n\t"
    "v_cvt_pk_bf16_f32 v232, v232, v233\n\t"
    "v_mul_f32 v236, v132, v68\n\t"
    "v_mfma_f32_16x16x32_bf16 v[100:103], a[120:123], a[144:147], v[100:103]\n\t"
    "v_mul_f32 v237, v133, v69\n\t"
    "v_cvt_pk_bf16_f32 v233, v234, v235\n\t"
    "v_mfma_f32_16x16x32_bf16 v[100:103], a[124:127], a[160:163], v[100:103]\n\t"
    "v_mul_f32 v238, v134, v70\n\t"
    "v_mul_f32 v239, v135, v71\n\t"
    "v_mfma_f32_16x16x32_bf16 v[104:107], a[96:99], v[200:203], v[104:107]\n\t"
    "v_cvt_pk_bf16_f32 v234, v236, v237\n\t"
    "v_cvt_pk_bf16_f32 v235, v238, v239\n\t"
    "v_mfma_f32_16x16x32_bf16 v[104:107], a[100:103], a[132:135], v[104:107]\n\t"
    "ds_write_b128 v226, v[232:235] offset:33280\n\t"
    "v_mfma_f32_16x16x32_bf16 v[104:107], a[104:107], a[148:151], v[104:107]\n\t"
    "v_mul_f32 v240, v136, v72\n\t"
    "v_mul_f32 v241, v137, v73\n\t"
    "v_mfma_f32_16x16x32_bf16 v[104:107], a[108:111], a[164:167], v[104:107]\n\t"
    "v_mul_f32 v242, v138, v74\n\t"
    "v_mul_f32 v243, v139, v75\n\t"
    "v_mfma_f32_16x16x32_bf16 v[108:111], a[112:115], v[200:203], v[108:111]\n\t"
    "s_waitcnt lgkmcnt(3)\n\t"
    "buffer_store_dwordx4 a[192:195], v251, s[44:47], s94 offen offset:64 sc1\n\t"
    "v_cvt_pk_bf16_f32 v240, v240, v241\n\t"
    "v_mfma_f32_16x16x32_bf16 v[108:111], a[116:119], a[132:135], v[108:111]\n\t"
    "v_mul_f32 v244, v140, v76\n\t"
    "v_mul_f32 v245, v141, v77\n\t"
    "v_mfma_f32_16x16x32_bf16 v[108:111], a[120:123], a[148:151], v[108:111]\n\t"
    "buffer_store_dwordx4 a[196:199], v251, s[44:47], s94 offen offset:2112 sc1\n\t"
    "v_cvt_pk_bf16_f32 v241, v242, v243\n\t"
    "v_mul_f32 v246, v142, v78\n\t"
    "v_mfma_f32_16x16x32_bf16 v[108:111], a[124:127], a[164:167], v[108:111]\n\t"
    "v_mul_f32 v247, v143, v79\n\t"
    "v_cvt_pk_bf16_f32 v242, v244, v245\n\t"
    "v_mfma_f32_16x16x32_bf16 v[112:115], a[96:99], v[208:211], v[112:115]\n\t"
    "s_waitcnt lgkmcnt(1)\n\t"
    "buffer_store_dwordx4 a[200:203], v251, s[44:47], s95 offen offset:64 sc1\n\t"
    "v_cvt_pk_bf16_f32 v243, v246, v247\n\t"
    "v_mfma_f32_16x16x32_bf16 v[112:115], a[100:103], a[136:139], v[112:115]\n\t"
    "ds_write_b128 v226, v[240:243] offset:41600\n\t"
    "v_mfma_f32_16x16x32_bf16 v[112:115], a[104:107], a[152:155], v[112:115]\n\t"
    "buffer_store_dwordx4 a[204:207], v251, s[44:47], s95 offen offset:2112 sc1\n\t"
    "v_mul_f32 v232, v144, v80\n\t"
    "v_mul_f32 v233, v145, v81\n\t"
    "v_mfma_f32_16x16x32_bf16 v[112:115], a[108:111], a[168:171], v[112:115]\n\t"
    "v_mul_f32 v234, v146, v82\n\t"
    "v_mul_f32 v235, v147, v83\n\t"
    "v_mfma_f32_16x16x32_bf16 v[116:119], a[112:115], v[208:211], v[116:119]\n\t"
    "v_cvt_pk_bf16_f32 v232, v232, v233\n\t"
    "v_mul_f32 v236, v148, v84\n\t"
    "v_mfma_f32_16x16x32_bf16 v[116:119], a[116:119], a[136:139], v[116:119]\n\t"
    "v_mul_f32 v237, v149, v85\n\t"
    "v_cvt_pk_bf16_f32 v233, v234, v235\n\t"
    "v_mfma_f32_16x16x32_bf16 v[116:119], a[120:123], a[152:155], v[116:119]\n\t"
    "v_mul_f32 v238, v150, v86\n\t"
    "v_mul_f32 v239, v151, v87\n\t"
    "v_mfma_f32_16x16x32_bf16 v[116:119], a[124:127], a[168:171], v[116:119]\n\t"
    "v_cvt_pk_bf16_f32 v234, v236, v237\n\t"
    "v_cvt_pk_bf16_f32 v235, v238, v239\n\t"
    "v_mfma_f32_16x16x32_bf16 v[120:123], a[96:99], v[216:219], v[120:123]\n\t"
    "ds_write_b128 v226, v[232:235] offset:49920\n\t"
    "v_mfma_f32_16x16x32_bf16 v[120:123], a[100:103], a[140:143], v[120:123]\n\t"
    "v_mul_f32 v240, v152, v88\n\t"
    "v_mul_f32 v241, v153, v89\n\t"
    "v_mfma_f32_16x16x32_bf16 v[120:123], a[104:107], a[156:159], v[120:123]\n\t"
    "v_mul_f32 v242, v154, v90\n\t"
    "v_mul_f32 v243, v155, v91\n\t"
    "v_mfma_f32_16x16x32_bf16 v[120:123], a[108:111], a[172:175], v[120:123]\n\t"
    "v_cvt_pk_bf16_f32 v240, v240, v241\n\t"
    "v_mul_f32 v244, v156, v92\n\t"
    "v_mfma_f32_16x16x32_bf16 v[124:127], a[112:115], v[216:219], v[124:127]\n\t"
    "v_mul_f32 v245, v157, v93\n\t"
    "v_cvt_pk_bf16_f32 v241, v242, v243\n\t"
    "v_mfma_f32_16x16x32_bf16 v[124:127], a[116:119], a[140:143], v[124:127]\n\t"
    "v_mul_f32 v246, v158, v94\n\t"
    "v_mul_f32 v247, v159, v95\n\t"
    "v_mfma_f32_16x16x32_bf16 v[124:127], a[120:123], a[156:159], v[124:127]\n\t"
    "v_cvt_pk_bf16_f32 v242, v244, v245\n\t"
    "v_cvt_pk_bf16_f32 v243, v246, v247\n\t"
    "v_mfma_f32_16x16x32_bf16 v[124:127], a[124:127], a[172:175], v[124:127]\n\t"
    "s_nop 7\n\t"
    "ds_write_b128 v226, v[240:243] offset:58240\n\t"
    "v_mul_f32 v232, v160, v96\n\t"
    "v_mul_f32 v233, v161, v97\n\t"
    "s_waitcnt lgkmcnt(0)\n\t"
    "s_barrier\n\t"
    "ds_read_b128 a[208:211], v249 offset:33280\n\t"
    "ds_read_b128 a[212:215], v249 offset:35360\n\t"
    "v_mul_f32 v234, v162, v98\n\t"
    "ds_read_b128 a[216:219], v249 offset:37440\n\t"
    "ds_read_b128 a[220:223], v249 offset:39520\n\t"
    "v_mul_f32 v235, v163, v99\n\t"
    "v_cvt_pk_bf16_f32 v232, v232, v233\n\t"
    "v_mul_f32 v236, v164, v100\n\t"
    "v_mul_f32 v237, v165, v101\n\t"
    "v_cvt_pk_bf16_f32 v233, v234, v235\n\t"
    "v_mul_f32 v238, v166, v102\n\t"
    "v_mul_f32 v239, v167, v103\n\t"
    "v_cvt_pk_bf16_f32 v234, v236, v237\n\t"
    "v_cvt_pk_bf16_f32 v235, v238, v239\n\t"
    "ds_write_b128 v226, v[232:235] offset:33344\n\t"
    "v_mul_f32 v240, v168, v104\n\t"
    "v_mul_f32 v241, v169, v105\n\t"
    "v_mul_f32 v242, v170, v106\n\t"
    "v_mul_f32 v243, v171, v107\n\t"
    "s_waitcnt lgkmcnt(3)\n\t"
    "buffer_store_dwordx4 a[208:211], v251, s[44:47], s94 offen offset:128 sc1\n\t"
    "v_cvt_pk_bf16_f32 v240, v240, v241\n\t"
    "v_mul_f32 v244, v172, v108\n\t"
    "v_mul_f32 v245, v173, v109\n\t"
    "buffer_store_dwordx4 a[212:215], v251, s[44:47], s94 offen offset:2176 sc1\n\t"
    "v_cvt_pk_bf16_f32 v241, v242, v243\n\t"
    "v_mul_f32 v246, v174, v110\n\t"
    "v_mul_f32 v247, v175, v111\n\t"
    "v_cvt_pk_bf16_f32 v242, v244, v245\n\t"
    "s_waitcnt lgkmcnt(1)\n\t"
    "buffer_store_dwordx4 a[216:219], v251, s[44:47], s95 offen offset:128 sc1\n\t"
    "v_cvt_pk_bf16_f32 v243, v246, v247\n\t"
    "ds_write_b128 v226, v[240:243] offset:41664\n\t"
    "buffer_store_dwordx4 a[220:223], v251, s[44:47], s95 offen offset:2176 sc1\n\t"
    "v_mul_f32 v232, v176, v112\n\t"
    "v_mul_f32 v233, v177, v113\n\t"
    "v_mul_f32 v234, v178, v114\n\t"
    "v_mul_f32 v235, v179, v115\n\t"
    "v_cvt_pk_bf16_f32 v232, v232, v233\n\t"
    "v_mul_f32 v236, v180, v116\n\t"
    "v_mul_f32 v237, v181, v117\n\t"
    "v_cvt_pk_bf16_f32 v233, v234, v235\n\t"
    "v_mul_f32 v238, v182, v118\n\t"
    "v_mul_f32 v239, v183, v119\n\t"
    "v_cvt_pk_bf16_f32 v234, v236, v237\n\t"
    "v_cvt_pk_bf16_f32 v235, v238, v239\n\t"
    "ds_write_b128 v226, v[232:235] offset:49984\n\t"
    "v_mul_f32 v240, v184, v120\n\t"
    "v_mul_f32 v241, v185, v121\n\t"
    "v_mul_f32 v242, v186, v122\n\t"
    "v_mul_f32 v243, v187, v123\n\t"
    "v_cvt_pk_bf16_f32 v240, v240, v241\n\t"
    "v_mul_f32 v244, v188, v124\n\t"
    "v_mul_f32 v245, v189, v125\n\t"
    "v_cvt_pk_bf16_f32 v241, v242, v243\n\t"
    "v_mul_f32 v246, v190, v126\n\t"
    "v_mul_f32 v247, v191, v127\n\t"
    "v_cvt_pk_bf16_f32 v242, v244, v245\n\t"
    "v_cvt_pk_bf16_f32 v243, v246, v247\n\t"
    "ds_write_b128 v226, v[240:243] offset:58304\n\t"
    "s_waitcnt lgkmcnt(0)\n\t"
    "s_barrier\n\t"
    "ds_read_b128 a[224:227], v249 offset:33344\n\t"
    "ds_read_b128 a[228:231], v249 offset:35424\n\t"
    "ds_read_b128 a[232:235], v249 offset:37504\n\t"
    "ds_read_b128 a[236:239], v249 offset:39584\n\t"
    "s_waitcnt lgkmcnt(2)\n\t"
    "buffer_store_dwordx4 a[224:227], v251, s[44:47], s94 offen offset:192 sc1\n\t"
    "buffer_store_dwordx4 a[228:231], v251, s[44:47], s94 offen offset:2240 sc1\n\t"
    "s_waitcnt lgkmcnt(0)\n\t"
    "buffer_store_dwordx4 a[232:235], v251, s[44:47], s95 offen offset:192 sc1\n\t"
    "buffer_store_dwordx4 a[236:239], v251, s[44:47], s95 offen offset:2240 sc1\n\t"
    "s_or_b32 s51, %3, %1\n\t"
    "s_cmp_lg_u32 s51, 0\n\t"
    "s_cbranch_scc1 Lnb%=\n\t"
    "s_cmp_eq_u32 s57, 0\n\t"
    "s_cbranch_scc1 Lnb%=\n\t"
    "v_mov_b32 v232, s56\n\t"
    "s_add_u32 s53, s58, 65536\n\t"
    "s_mov_b64 exec, 1\n\t"
    "buffer_store_dword v232, off, s[64:67], s53 sc1\n\t"
    "s_mov_b64 exec, -1\n\t"
    "Lnb%=:\n\t"
    "s_branch Lend%=\n\t"
    "Lloop%=:\n\t"
    "s_waitcnt vmcnt(22) lgkmcnt(5)\n\t"
    "v_mfma_f32_16x16x32_bf16 v[192:195], a[0:3], a[176:179], 0\n\t"
    "ds_read_b128 a[200:203], v225 offset:16656\n\t"
    "buffer_load_dwordx4 a[104:107], v224, s[36:39], s48 offen offset:2048 sc1\n\t"
    "v_mul_f32 v244, v156, v92\n\t"
    "v_mfma_f32_16x16x32_bf16 v[196:199], a[4:7], a[176:179], 0\n\t"
    "ds_read_b128 a[204:207], v225 offset:24976\n\t"
    "v_mul_f32 v245, v157, v93\n\t"
    "v_cvt_pk_bf16_f32 v241, v242, v243\n\t"
    "v_mfma_f32_16x16x32_bf16 v[200:203], a[0:3], a[180:183], 0\n\t"
    "v_mul_f32 v246, v158, v94\n\t"
    "v_mul_f32 v247, v159, v95\n\t"
    "v_mfma_f32_16x16x32_bf16 v[204:207], a[4:7], a[180:183], 0\n\t"
    "s_waitcnt lgkmcnt(5)\n\t"
    "v_cvt_pk_bf16_f32 v242, v244, v245\n\t"
    "v_cvt_pk_bf16_f32 v243, v246, v247\n\t"
    "v_mfma_f32_16x16x32_bf16 v[208:211], a[0:3], a[184:187], 0\n\t"
    "ds_read_b128 a[208:211], v225 offset:32\n\t"
    "buffer_load_dwordx4 a[108:111], v224, s[36:39], s48 offen offset:3072 sc1\n\t"
    "s_add_u32 s48, s48, 4096\n\t"
    "v_mfma_f32_16x16x32_bf16 v[212:215], a[4:7], a[184:187], 0\n\t"
    "ds_read_b128 a[212:215], v225 offset:8352\n\t"
    "v_add_f32 v11, v251, v11\n\t"
    "v_add_f32 v16, v248, v16\n\t"
    "v_mfma_f32_16x16x32_bf16 v[216:219], a[0:3], a[188:191], 0\n\t"
    "ds_write_b128 v226, v[240:243] offset:58240\n\t"
    "v_mfma_f32_16x16x32_bf16 v[220:223], a[4:7], a[188:191], 0\n\t"
    "s_waitcnt lgkmcnt(5)\n\t"
    "v_mul_f32 v232, v160, v96\n\t"
    "v_mul_f32 v233, v161, v97\n\t"
    "v_mfma_f32_16x16x32_bf16 v[192:195], a[8:11], a[192:195], v[192:195]\n\t"
    "ds_read_b128 a[216:219], v225 offset:16672\n\t"
    "buffer_load_dwordx4 a[112:115], v224, s[36:39], s48 offen offset:0 sc1\n\t"
    "v_mul_f32 v234, v162, v98\n\t"
    "v_mfma_f32_16x16x32_bf16 v[196:199], a[12:15], a[192:195], v[196:199]\n\t"
    "ds_read_b128 a[220:223], v225 offset:24992\n\t"
    "v_mul_f32 v235, v163, v99\n\t"
    "v_cvt_pk_bf16_f32 v232, v232, v233\n\t"
    "v_mfma_f32_16x16x32_bf16 v[200:203], a[8:11], a[196:199], v[200:203]\n\t"
    "v_mul_f32 v236, v164, v100\n\t"
    "v_mul_f32 v237, v165, v101\n\t"
    "v_mfma_f32_16x16x32_bf16 v[204:207], a[12:15], a[196:199], v[204:207]\n\t"
    "s_waitcnt lgkmcnt(5)\n\t"
    "v_cvt_pk_bf16_f32 v233, v234, v235\n\t"
    "v_mul_f32 v238, v166, v102\n\t"
    "v_mfma_f32_16x16x32_bf16 v[208:211], a[8:11], a[200:203], v[208:211]\n\t"
    "ds_read_b128 a[224:227], v225 offset:48\n\t"
    "buffer_load_dwordx4 a[116:119], v224, s[36:39], s48 offen offset:1024 sc1\n\t"
    "v_mul_f32 v239, v167, v103\n\t"
    "v_mfma_f32_16x16x32_bf16 v[212:215], a[12:15], a[200:203], v[212:215]\n\t"
    "ds_read_b128 a[228:231], v225 offset:8368\n\t"
    "v_cvt_pk_bf16_f32 v234, v236, v237\n\t"
    "v_cvt_pk_bf16_f32 v235, v238, v239\n\t"
    "v_mfma_f32_16x16x32_bf16 v[216:219], a[8:11], a[204:207], v[216:219]\n\t"
    "ds_write_b128 v226, v[232:235] offset:33344\n\t"
    "v_mfma_f32_16x16x32_bf16 v[220:223], a[12:15], a[204:207], v[220:223]\n\t"
    "s_waitcnt vmcnt(22) lgkmcnt(6)\n\t"
    "v_mul_f32 v240, v168, v104\n\t"
    "v_mfma_f32_16x16x32_bf16 v[192:195], a[16:19], a[208:211], v[192:195]\n\t"
    "ds_read_b128 a[176:179], v225 offset:16688\n\t"
    "buffer_load_dwordx4 a[120:123], v224, s[36:39], s48 offen offset:2048 sc1\n\t"
    "v_mul_f32 v241, v169, v105\n\t"
    "v_mfma_f32_16x16x32_bf16 v[196:199], a[20:23], a[208:211], v[196:199]\n\t"
    "ds_read_b128 a[180:183], v225 offset:25008\n\t"
    "v_mul_f32 v242, v170, v106\n\t"
    "v_mul_f32 v243, v171, v107\n\t"
    "v_mfma_f32_16x16x32_bf16 v[200:203], a[16:19], a[212:215], v[200:203]\n\t"
    "v_cvt_pk_bf16_f32 v240, v240, v241\n\t"
    "v_mul_f32 v244, v172, v108\n\t"
    "v_mfma_f32_16x16x32_bf16 v[204:207], a[20:23], a[212:215], v[204:207]\n\t"
    "s_waitcnt lgkmcnt(5)\n\t"
    "v_mul_f32 v245, v173, v109\n\t"
    "v_cvt_pk_bf16_f32 v241, v242, v243\n\t"
    "v_mfma_f32_16x16x32_bf16 v[208:211], a[16:19], a[216:219], v[208:211]\n\t"
    "ds_read_b128 a[184:187], v225 offset:64\n\t"
    "buffer_load_dwordx4 a[124:127], v224, s[36:39], s48 offen offset:3072 sc1\n\t"
    "s_add_u32 s48, s48, 4096\n\t"
    "v_mfma_f32_16x16x32_bf16 v[212:215], a[20:23], a[216:219], v[212:215]\n\t"
    "ds_read_b128 a[188:191], v225 offset:8384\n\t"
    "v_mul_f32 v246, v174, v110\n\t"
    "v_mul_f32 v247, v175, v111\n\t"
    "v_mfma_f32_16x16x32_bf16 v[216:219], a[16:19], a[220:223], v[216:219]\n\t"
    "v_cvt_pk_bf16_f32 v242, v244, v245\n\t"
    "v_cvt_pk_bf16_f32 v243, v246, v247\n\t"
    "v_mfma_f32_16x16x32_bf16 v[220:223], a[20:23], a[220:223], v[220:223]\n\t"
    "s_waitcnt lgkmcnt(5)\n\t"
    "ds_write_b128 v226, v[240:243] offset:41664\n\t"
    "v_mfma_f32_16x16x32_bf16 v[192:195], a[24:27], a[224:227], v[192:195]\n\t"
    "ds_read_b128 a[192:195], v225 offset:16704\n\t"
    "buffer_load_dwordx4 a[0:3], v224, s[36:39], s48 offen offset:0 sc1\n\t"
    "v_mul_f32 v232, v176, v112\n\t"
    "v_mfma_f32_16x16x32_bf16 v[196:199], a[28:31], a[224:227], v[196:199]\n\t"
    "ds_read_b128 a[196:199], v225 offset:25024\n\t"
    "v_mul_f32 v233, v177, v113\n\t"
    "v_mul_f32 v234, v178, v114\n\t"
    "v_mfma_f32_16x16x32_bf16 v[200:203], a[24:27], a[228:231], v[200:203]\n\t"
    "v_mul_f32 v235, v179, v115\n\t"
    "v_cvt_pk_bf16_f32 v232, v232, v233\n\t"
    "v_mfma_f32_16x16x32_bf16 v[204:207], a[28:31], a[228:231], v[204:207]\n\t"
    "s_waitcnt lgkmcnt(5)\n\t"
    "v_mul_f32 v236, v180, v116\n\t"
    "v_mul_f32 v237, v181, v117\n\t"
    "v_mfma_f32_16x16x32_bf16 v[208:211], a[24:27], a[176:179], v[208:211]\n\t"
    "ds_read_b128 a[200:203], v225 offset:80\n\t"
    "buffer_load_dwordx4 a[4:7], v224, s[36:39], s48 offen offset:1024 sc1\n\t"
    "v_cvt_pk_bf16_f32 v233, v234, v235\n\t"
    "v_mfma_f32_16x16x32_bf16 v[212:215], a[28:31], a[176:179], v[212:215]\n\t"
    "ds_read_b128 a[204:207], v225 offset:8400\n\t"
    "v_mul_f32 v238, v182, v118\n\t"
    "v_mul_f32 v239, v183, v119\n\t"
    "v_mfma_f32_16x16x32_bf16 v[216:219], a[24:27], a[180:183], v[216:219]\n\t"
    "v_cvt_pk_bf16_f32 v234, v236, v237\n\t"
    "v_cvt_pk_bf16_f32 v235, v238, v239\n\t"
    "v_mfma_f32_16x16x32_bf16 v[220:223], a[28:31], a[180:183], v[220:223]\n\t"
    "s_waitcnt vmcnt(22) lgkmcnt(5)\n\t"
    "ds_write_b128 v226, v[232:235] offset:49984\n\t"
    "v_mfma_f32_16x16x32_bf16 v[192:195], a[32:35], a[184:187], v[192:195]\n\t"
    "ds_read_b128 a[208:211], v225 offset:16720\n\t"
    "buffer_load_dwordx4 a[8:11], v224, s[36:39], s48 offen offset:2048 sc1\n\t"
    "v_mul_f32 v240, v184, v120\n\t"
    "v_mfma_f32_16x16x32_bf16 v[196:199], a[36:39], a[184:187], v[196:199]\n\t"
    "ds_read_b128 a[212:215], v225 offset:25040\n\t"
    "v_mul_f32 v241, v185, v121\n\t"
    "v_mul_f32 v242, v186, v122\n\t"
    "v_mfma_f32_16x16x32_bf16 v[200:203], a[32:35], a[188:191], v[200:203]\n\t"
    "v_mul_f32 v243, v187, v123\n\t"
    "v_cvt_pk_bf16_f32 v240, v240, v241\n\t"
    "v_mfma_f32_16x16x32_bf16 v[204:207], a[36:39], a[188:191], v[204:207]\n\t"
    "s_waitcnt lgkmcnt(5)\n\t"
    "v_mul_f32 v244, v188, v124\n\t"
    "v_mul_f32 v245, v189, v125\n\t"
    "v_mfma_f32_16x16x32_bf16 v[208:211], a[32:35], a[192:195], v[208:211]\n\t"
    "ds_read_b128 a[216:219], v225 offset:96\n\t"
    "buffer_load_dwordx4 a[12:15], v224, s[36:39], s48 offen offset:3072 sc1\n\t"
    "s_add_u32 s48, s48, 4096\n\t"
    "v_mfma_f32_16x16x32_bf16 v[212:215], a[36:39], a[192:195], v[212:215]\n\t"
    "ds_read_b128 a[220:223], v225 offset:8416\n\t"
    "v_cvt_pk_bf16_f32 v241, v242, v243\n\t"
    "v_mul_f32 v246, v190, v126\n\t"
    "v_mfma_f32_16x16x32_bf16 v[216:219], a[32:35], a[196:199], v[216:219]\n\t"
    "v_mul_f32 v247, v191, v127\n\t"
    "v_cvt_pk_bf16_f32 v242, v244, v245\n\t"
    "v_mfma_f32_16x16x32_bf16 v[220:223], a[36:39], a[196:199], v[220:223]\n\t"
    "s_waitcnt lgkmcnt(5)\n\t"
    "v_cvt_pk_bf16_f32 v243, v246, v247\n\t"
    "v_add_f32 v17, v249, v17\n\t"
    "v_mfma_f32_16x16x32_bf16 v[192:195], a[40:43], a[200:203], v[192:195]\n\t"
    "ds_read_b128 a[224:227], v225 offset:16736\n\t"
    "buffer_load_dwordx4 a[16:19], v224, s[36:39], s48 offen offset:0 sc1\n\t"
    "v_add_f32 v18, v250, v18\n\t"
    "v_mfma_f32_16x16x32_bf16 v[196:199], a[44:47], a[200:203], v[196:199]\n\t"
    "ds_read_b128 a[228:231], v225 offset:25056\n\t"
    "v_add_f32 v19, v251, v19\n\t"
    "v_add_f32 v24, v248, v24\n\t"
    "v_mfma_f32_16x16x32_bf16 v[200:203], a[40:43], a[204:207], v[200:203]\n\t"
    "ds_write_b128 v226, v[240:243] offset:58304\n\t"
    "v_mfma_f32_16x16x32_bf16 v[204:207], a[44:47], a[204:207], v[204:207]\n\t"
    "s_waitcnt lgkmcnt(5)\n\t"
    "v_add_f32 v25, v249, v25\n\t"
    "v_add_f32 v26, v250, v26\n\t"
    "v_mfma_f32_16x16x32_bf16 v[208:211], a[40:43], a[208:211], v[208:211]\n\t"
    "ds_read_b128 a[176:179], v225 offset:112\n\t"
    "buffer_load_dwordx4 a[20:23], v224, s[36:39], s48 offen offset:1024 sc1\n\t"
    "v_add_f32 v27, v251, v27\n\t"
    "v_mfma_f32_16x16x32_bf16 v[212:215], a[44:47], a[208:211], v[212:215]\n\t"
    "ds_read_b128 a[180:183], v225 offset:8432\n\t"
    "ds_read_b128 v[248:251], v230 offset:128\n\t"
    "v_add_f32 v4, v252, v4\n\t"
    "v_mfma_f32_16x16x32_bf16 v[216:219], a[40:43], a[212:215], v[216:219]\n\t"
    "v_add_f32 v5, v253, v5\n\t"
    "v_add_f32 v6, v254, v6\n\t"
    "v_mfma_f32_16x16x32_bf16 v[220:223], a[44:47], a[212:215], v[220:223]\n\t"
    "s_waitcnt vmcnt(22) lgkmcnt(6)\n\t"
    "v_add_f32 v7, v255, v7\n\t"
    "v_mfma_f32_16x16x32_bf16 v[192:195], a[48:51], a[216:219], v[192:195]\n\t"
    "ds_read_b128 a[184:187], v225 offset:16752\n\t"
    "buffer_load_dwordx4 a[24:27], v224, s[36:39], s48 offen offset:2048 sc1\n\t"
    "v_add_f32 v12, v252, v12\n\t"
    "v_mfma_f32_16x16x32_bf16 v[196:199], a[52:55], a[216:219], v[196:199]\n\t"
    "ds_read_b128 a[188:191], v225 offset:25072\n\t"
    "v_add_f32 v13, v253, v13\n\t"
    "v_add_f32 v14, v254, v14\n\t"
    "v_mfma_f32_16x16x32_bf16 v[200:203], a[48:51], a[220:223], v[200:203]\n\t"
    "v_add_f32 v15, v255, v15\n\t"
    "v_add_f32 v20, v252, v20\n\t"
    "v_mfma_f32_16x16x32_bf16 v[204:207], a[52:55], a[220:223], v[204:207]\n\t"
    "s_waitcnt lgkmcnt(5)\n\t"
    "s_barrier\n\t"
    "v_mfma_f32_16x16x32_bf16 v[208:211], a[48:51], a[224:227], v[208:211]\n\t"
    "ds_read_b128 a[192:195], v225 offset:33280\n\t"
    "buffer_load_dwordx4 a[28:31], v224, s[36:39], s48 offen offset:3072 sc1\n\t"
    "s_add_u32 s48, s48, 4096\n\t"
    "v_mfma_f32_16x16x32_bf16 v[212:215], a[52:55], a[224:227], v[212:215]\n\t"
    "ds_read_b128 a[196:199], v225 offset:41600\n\t"
    "v_add_f32 v21, v253, v21\n\t"
    "v_add_f32 v22, v254, v22\n\t"
    "v_mfma_f32_16x16x32_bf16 v[216:219], a[48:51], a[228:231], v[216:219]\n\t"
    "v_add_f32 v23, v255, v23\n\t"
    "v_add_f32 v28, v252, v28\n\t"
    "v_mfma_f32_16x16x32_bf16 v[220:223], a[52:55], a[228:231], v[220:223]\n\t"
    "s_waitcnt lgkmcnt(5)\n\t"
    "v_add_f32 v29, v253, v29\n\t"
    "v_add_f32 v30, v254, v30\n\t"
    "v_mfma_f32_16x16x32_bf16 v[192:195], a[56:59], a[176:179], v[192:195]\n\t"
    "ds_read_b128 a[200:203], v225 offset:49920\n\t"
    "buffer_load_dwordx4 a[32:35], v224, s[36:39], s48 offen offset:0 sc1\n\t"
    "v_add_f32 v31, v255, v31\n\t"
    "v_mfma_f32_16x16x32_bf16 v[196:199], a[60:63], a[176:179], v[196:199]\n\t"
    "s_waitcnt lgkmcnt(5)\n\t"
    "ds_read_b128 a[204:207], v225 offset:58240\n\t"
    "ds_read_b128 v[252:255], v230 offset:144\n\t"
    "v_add_f32 v32, v248, v32\n\t"
    "v_mfma_f32_16x16x32_bf16 v[200:203], a[56:59], a[180:183], v[200:203]\n\t"
    "v_add_f32 v33, v249, v33\n\t"
    "v_add_f32 v34, v250, v34\n\t"
    "v_mfma_f32_16x16x32_bf16 v[204:207], a[60:63], a[180:183], v[204:207]\n\t"
    "s_waitcnt lgkmcnt(5)\n\t"
    "v_add_f32 v35, v251, v35\n\t"
    "v_add_f32 v40, v248, v40\n\t"
    "v_mfma_f32_16x16x32_bf16 v[208:211], a[56:59], a[184:187], v[208:211]\n\t"
    "ds_read_b128 a[208:211], v225 offset:33296\n\t"
    "buffer_load_dwordx4 a[36:39], v224, s[36:39], s48 offen offset:1024 sc1\n\t"
    "v_add_f32 v41, v249, v41\n\t"
    "v_mfma_f32_16x16x32_bf16 v[212:215], a[60:63], a[184:187], v[212:215]\n\t"
    "ds_read_b128 a[212:215], v225 offset:41616\n\t"
    "v_add_f32 v42, v250, v42\n\t"
    "v_add_f32 v43, v251, v43\n\t"
    "v_mfma_f32_16x16x32_bf16 v[216:219], a[56:59], a[188:191], v[216:219]\n\t"
    "v_add_f32 v48, v248, v48\n\t"
    "v_add_f32 v49, v249, v49\n\t"
    "v_mfma_f32_16x16x32_bf16 v[220:223], a[60:63], a[188:191], v[220:223]\n\t"
    "s_waitcnt vmcnt(22) lgkmcnt(5)\n\t"
    "v_add_f32 v50, v250, v50\n\t"
    "v_mfma_f32_16x16x32_bf16 v[192:195], a[64:67], a[192:195], v[192:195]\n\t"
    "ds_read_b128 a[216:219], v225 offset:49936\n\t"
    "buffer_load_dwordx4 a[40:43], v224, s[36:39], s48 offen offset:2048 sc1\n\t"
    "v_add_f32 v51, v251, v51\n\t"
    "v_mfma_f32_16x16x32_bf16 v[196:199], a[68:71], a[192:195], v[196:199]\n\t"
    "ds_read_b128 a[220:223], v225 offset:58256\n\t"
    "v_add_f32 v56, v248, v56\n\t"
    "v_add_f32 v57, v249, v57\n\t"
    "v_mfma_f32_16x16x32_bf16 v[200:203], a[64:67], a[196:199], v[200:203]\n\t"
    "v_add_f32 v58, v250, v58\n\t"
    "v_add_f32 v59, v251, v59\n\t"
    "v_mfma_f32_16x16x32_bf16 v[204:207], a[68:71], a[196:199], v[204:207]\n\t"
    "s_waitcnt lgkmcnt(4)\n\t"
    "ds_read_b128 v[248:251], v230 offset:256\n\t"
    "v_add_f32 v36, v252, v36\n\t"
    "v_mfma_f32_16x16x32_bf16 v[208:211], a[64:67], a[200:203], v[208:211]\n\t"
    "ds_read_b128 a[224:227], v225 offset:33312\n\t"
    "buffer_load_dwordx4 a[44:47], v224, s[36:39], s48 offen offset:3072 sc1\n\t"
    "s_add_u32 s48, s48, 4096\n\t"
    "v_mfma_f32_16x16x32_bf16 v[212:215], a[68:71], a[200:203], v[212:215]\n\t"
    "ds_read_b128 a[228:231], v225 offset:41632\n\t"
    "v_add_f32 v37, v253, v37\n\t"
    "v_add_f32 v38, v254, v38\n\t"
    "v_mfma_f32_16x16x32_bf16 v[216:219], a[64:67], a[204:207], v[216:219]\n\t"
    "v_add_f32 v39, v255, v39\n\t"
    "v_add_f32 v44, v252, v44\n\t"
    "v_mfma_f32_16x16x32_bf16 v[220:223], a[68:71], a[204:207], v[220:223]\n\t"
    "s_waitcnt lgkmcnt(5)\n\t"
    "v_add_f32 v45, v253, v45\n\t"
    "v_add_f32 v46, v254, v46\n\t"
    "v_mfma_f32_16x16x32_bf16 v[192:195], a[72:75], a[208:211], v[192:195]\n\t"
    "ds_read_b128 a[176:179], v225 offset:49952\n\t"
    "buffer_load_dwordx4 a[48:51], v224, s[36:39], s48 offen offset:0 sc1\n\t"
    "v_add_f32 v47, v255, v47\n\t"
    "v_mfma_f32_16x16x32_bf16 v[196:199], a[76:79], a[208:211], v[196:199]\n\t"
    "ds_read_b128 a[180:183], v225 offset:58272\n\t"
    "v_add_f32 v52, v252, v52\n\t"
    "v_add_f32 v53, v253, v53\n\t"
    "v_mfma_f32_16x16x32_bf16 v[200:203], a[72:75], a[212:215], v[200:203]\n\t"
    "v_add_f32 v54, v254, v54\n\t"
    "v_add_f32 v55, v255, v55\n\t"
    "v_mfma_f32_16x16x32_bf16 v[204:207], a[76:79], a[212:215], v[204:207]\n\t"
    "s_waitcnt lgkmcnt(5)\n\t"
    "v_add_f32 v60, v252, v60\n\t"
    "v_add_f32 v61, v253, v61\n\t"
    "v_mfma_f32_16x16x32_bf16 v[208:211], a[72:75], a[216:219], v[208:211]\n\t"
    "ds_read_b128 a[184:187], v225 offset:33328\n\t"
    "buffer_load_dwordx4 a[52:55], v224, s[36:39], s48 offen offset:1024 sc1\n\t"
    "v_add_f32 v62, v254, v62\n\t"
    "v_mfma_f32_16x16x32_bf16 v[212:215], a[76:79], a[216:219], v[212:215]\n\t"
    "ds_read_b128 a[188:191], v225 offset:41648\n\t"
    "v_add_f32 v63, v255, v63\n\t"
    "v_mfma_f32_16x16x32_bf16 v[216:219], a[72:75], a[220:223], v[216:219]\n\t"
    "s_waitcnt lgkmcnt(6)\n\t"
    "ds_read_b128 v[252:255], v230 offset:272\n\t"
    "v_add_f32 v64, v248, v64\n\t"
    "v_add_f32 v65, v249, v65\n\t"
    "v_mfma_f32_16x16x32_bf16 v[220:223], a[76:79], a[220:223], v[220:223]\n\t"
    "s_waitcnt vmcnt(22) lgkmcnt(5)\n\t"
    "v_add_f32 v66, v250, v66\n\t"
    "v_mfma_f32_16x16x32_bf16 v[192:195], a[80:83], a[224:227], v[192:195]\n\t"
    "ds_read_b128 a[192:195], v225 offset:49968\n\t"
    "buffer_load_dwordx4 a[56:59], v224, s[36:39], s48 offen offset:2048 sc1\n\t"
    "v_add_f32 v67, v251, v67\n\t"
    "v_mfma_f32_16x16x32_bf16 v[196:199], a[84:87], a[224:227], v[196:199]\n\t"
    "ds_read_b128 a[196:199], v225 offset:58288\n\t"
    "v_add_f32 v72, v248, v72\n\t"
    "v_add_f32 v73, v249, v73\n\t"
    "v_mfma_f32_16x16x32_bf16 v[200:203], a[80:83], a[228:231], v[200:203]\n\t"
    "v_add_f32 v74, v250, v74\n\t"
    "v_add_f32 v75, v251, v75\n\t"
    "v_mfma_f32_16x16x32_bf16 v[204:207], a[84:87], a[228:231], v[204:207]\n\t"
    "s_waitcnt lgkmcnt(5)\n\t"
    "v_add_f32 v80, v248, v80\n\t"
    "v_add_f32 v81, v249, v81\n\t"
    "v_mfma_f32_16x16x32_bf16 v[208:211], a[80:83], a[176:179], v[208:211]\n\t"
    "ds_read_b128 a[200:203], v225 offset:33344\n\t"
    "buffer_load_dwordx4 a[60:63], v224, s[36:39], s48 offen offset:3072 sc1\n\t"
    "s_add_u32 s48, s48, 4096\n\t"
    "v_mfma_f32_16x16x32_bf16 v[212:215], a[84:87], a[176:179], v[212:215]\n\t"
    "ds_read_b128 a[204:207], v225 offset:33360\n\t"
    "v_add_f32 v82, v250, v82\n\t"
    "v_add_f32 v83, v251, v83\n\t"
    "v_mfma_f32_16x16x32_bf16 v[216:219], a[80:83], a[180:183], v[216:219]\n\t"
    "v_add_f32 v88, v248, v88\n\t"
    "v_add_f32 v89, v249, v89\n\t"
    "v_mfma_f32_16x16x32_bf16 v[220:223], a[84:87], a[180:183], v[220:223]\n\t"
    "s_waitcnt lgkmcnt(5)\n\t"
    "v_add_f32 v90, v250, v90\n\t"
    "v_add_f32 v91, v251, v91\n\t"
    "v_mfma_f32_16x16x32_bf16 v[192:195], a[88:91], a[184:187], v[192:195]\n\t"
    "s_waitcnt lgkmcnt(4)\n\t"
    "ds_read_b128 a[208:211], v225 offset:33376\n\t"
    "buffer_load_dwordx4 a[64:67], v224, s[36:39], s48 offen offset:0 sc1\n\t"
    "v_add_f32 v68, v252, v68\n\t"
    "v_mfma_f32_16x16x32_bf16 v[196:199], a[92:95], a[184:187], v[196:199]\n\t"
    "ds_read_b128 a[212:215], v225 offset:33392\n\t"
    "ds_read_b128 v[248:251], v230 offset:384\n\t"
    "v_add_f32 v69, v253, v69\n\t"
    "v_mfma_f32_16x16x32_bf16 v[200:203], a[88:91], a[188:191], v[200:203]\n\t"
    "v_add_f32 v70, v254, v70\n\t"
    "v_add_f32 v71, v255, v71\n\t"
    "v_mfma_f32_16x16x32_bf16 v[204:207], a[92:95], a[188:191], v[204:207]\n\t"
    "s_waitcnt lgkmcnt(5)\n\t"
    "v_add_f32 v76, v252, v76\n\t"
    "v_add_f32 v77, v253, v77\n\t"
    "v_mfma_f32_16x16x32_bf16 v[208:211], a[88:91], a[192:195], v[208:211]\n\t"
    "ds_read_b128 a[216:219], v225 offset:41664\n\t"
    "buffer_load_dwordx4 a[68:71], v224, s[36:39], s48 offen offset:1024 sc1\n\t"
    "v_add_f32 v78, v254, v78\n\t"
    "v_mfma_f32_16x16x32_bf16 v[212:215], a[92:95], a[192:195], v[212:215]\n\t"
    "ds_read_b128 a[220:223], v225 offset:41680\n\t"
    "v_add_f32 v79, v255, v79\n\t"
    "v_add_f32 v84, v252, v84\n\t"
    "v_mfma_f32_16x16x32_bf16 v[216:219], a[88:91], a[196:199], v[216:219]\n\t"
    "v_add_f32 v85, v253, v85\n\t"
    "v_add_f32 v86, v254, v86\n\t"
    "v_mfma_f32_16x16x32_bf16 v[220:223], a[92:95], a[196:199], v[220:223]\n\t"
    "s_waitcnt vmcnt(22) lgkmcnt(5)\n\t"
    "v_add_f32 v87, v255, v87\n\t"
    "v_mfma_f32_16x16x32_bf16 v[192:195], a[96:99], a[200:203], v[192:195]\n\t"
    "ds_read_b128 a[224:227], v225 offset:41696\n\t"
    "buffer_load_dwordx4 a[72:75], v224, s[36:39], s48 offen offset:2048 sc1\n\t"
    "v_add_f32 v92, v252, v92\n\t"
    "v_mfma_f32_16x16x32_bf16 v[196:199], a[100:103], a[200:203], v[196:199]\n\t"
    "ds_read_b128 a[228:231], v225 offset:41712\n\t"
    "v_add_f32 v93, v253, v93\n\t"
    "v_add_f32 v94, v254, v94\n\t"
    "v_mfma_f32_16x16x32_bf16 v[192:195], a[104:107], a[204:207], v[192:195]\n\t"
    "s_waitcnt lgkmcnt(4)\n\t"
    "v_add_f32 v95, v255, v95\n\t"
    "v_add_f32 v96, v248, v96\n\t"
    "v_mfma_f32_16x16x32_bf16 v[196:199], a[108:111], a[204:207], v[196:199]\n\t"
    "s_waitcnt vmcnt(19)\n\t"
    "v_add_f32 v97, v249, v97\n\t"
    "v_mfma_f32_16x16x32_bf16 v[192:195], a[112:115], a[208:211], v[192:195]\n\t"
    "ds_read_b128 a[176:179], v225 offset:49984\n\t"
    "buffer_load_dwordx4 a[76:79], v224, s[36:39], s48 offen offset:3072 sc1\n\t"
    "s_add_u32 s48, s48, 4096\n\t"
    "v_mfma_f32_16x16x32_bf16 v[196:199], a[116:119], a[208:211], v[196:199]\n\t"
    "ds_read_b128 a[180:183], v225 offset:50000\n\t"
    "ds_read_b128 v[252:255], v230 offset:400\n\t"
    "v_add_f32 v98, v250, v98\n\t"
    "v_mfma_f32_16x16x32_bf16 v[192:195], a[120:123], a[212:215], v[192:195]\n\t"
    "v_add_u32 v230, 512, v230\n\t"
    "v_add_f32 v99, v251, v99\n\t"
    "v_mfma_f32_16x16x32_bf16 v[196:199], a[124:127], a[212:215], v[196:199]\n\t"
    "s_waitcnt lgkmcnt(5)\n\t"
    "v_add_f32 v104, v248, v104\n\t"
    "v_add_f32 v105, v249, v105\n\t"
    "v_mfma_f32_16x16x32_bf16 v[200:203], a[96:99], a[216:219], v[200:203]\n\t"
    "ds_read_b128 a[184:187], v225 offset:50016\n\t"
    "buffer_load_dwordx4 a[80:83], v224, s[36:39], s48 offen offset:0 sc1\n\t"
    "v_add_f32 v106, v250, v106\n\t"
    "v_mfma_f32_16x16x32_bf16 v[204:207], a[100:103], a[216:219], v[204:207]\n\t"
    "ds_read_b128 a[188:191], v225 offset:50032\n\t"
    "v_add_f32 v107, v251, v107\n\t"
    "v_add_f32 v112, v248, v112\n\t"
    "v_mfma_f32_16x16x32_bf16 v[200:203], a[104:107], a[220:223], v[200:203]\n\t"
    "v_cvt_pk_bf16_f32 v192, v192, v193\n\t"
    "v_cvt_pk_bf16_f32 v193, v194, v195\n\t"
    "v_mfma_f32_16x16x32_bf16 v[204:207], a[108:111], a[220:223], v[204:207]\n\t"
    "s_waitcnt lgkmcnt(5)\n\t"
    "v_cvt_pk_bf16_f32 v194, v196, v197\n\t"
    "v_cvt_pk_bf16_f32 v195, v198, v199\n\t"
    "v_mfma_f32_16x16x32_bf16 v[200:203], a[112:115], a[224:227], v[200:203]\n\t"
    "ds_read_b128 a[192:195], v225 offset:58304\n\t"
    "buffer_load_dwordx4 a[84:87], v224, s[36:39], s48 offen offset:1024 sc1\n\t"
    "v_add_f32 v113, v249, v113\n\t"
    "v_mfma_f32_16x16x32_bf16 v[204:207], a[116:119], a[224:227], v[204:207]\n\t"
    "ds_read_b128 a[196:199], v225 offset:58320\n\t"
    "v_add_f32 v114, v250, v114\n\t"
    "v_add_f32 v115, v251, v115\n\t"
    "v_mfma_f32_16x16x32_bf16 v[200:203], a[120:123], a[228:231], v[200:203]\n\t"
    "ds_write_b128 v224, v[192:195] offset:0\n\t"
    "v_mfma_f32_16x16x32_bf16 v[204:207], a[124:127], a[228:231], v[204:207]\n\t"
    "s_waitcnt lgkmcnt(6)\n\t"
    "v_add_f32 v120, v248, v120\n\t"
    "v_add_f32 v121, v249, v121\n\t"
    "v_mfma_f32_16x16x32_bf16 v[208:211], a[96:99], a[176:179], v[208:211]\n\t"
    "ds_read_b128 a[200:203], v225 offset:58336\n\t"
    "buffer_load_dwordx4 a[88:91], v224, s[36:39], s48 offen offset:2048 sc1\n\t"
    "v_add_f32 v122, v250, v122\n\t"
    "v_mfma_f32_16x16x32_bf16 v[212:215], a[100:103], a[176:179], v[212:215]\n\t"
    "ds_read_b128 a[204:207], v225 offset:58352\n\t"
    "v_add_f32 v123, v251, v123\n\t"
    "v_mfma_f32_16x16x32_bf16 v[208:211], a[104:107], a[180:183], v[208:211]\n\t"
    "v_cvt_pk_bf16_f32 v200, v200, v201\n\t"
    "v_cvt_pk_bf16_f32 v201, v202, v203\n\t"
    "v_mfma_f32_16x16x32_bf16 v[212:215], a[108:111], a[180:183], v[212:215]\n\t"
    "s_waitcnt lgkmcnt(5)\n\t"
    "v_cvt_pk_bf16_f32 v202, v204, v205\n\t"
    "v_cvt_pk_bf16_f32 v203, v206, v207\n\t"
    "v_mfma_f32_16x16x32_bf16 v[208:211], a[112:115], a[184:187], v[208:211]\n\t"
    "ds_write_b128 v224, v[200:203] offset:1024\n\t"
    "v_mfma_f32_16x16x32_bf16 v[212:215], a[116:119], a[184:187], v[212:215]\n\t"
    "buffer_load_dwordx4 a[92:95], v224, s[36:39], s48 offen offset:3072 sc1\n\t"
    "s_add_u32 s48, s48, 4096\n\t"
    "v_add_f32 v100, v252, v100\n\t"
    "v_mfma_f32_16x16x32_bf16 v[208:211], a[120:123], a[188:191], v[208:211]\n\t"
    "v_add_f32 v101, v253, v101\n\t"
    "v_add_f32 v102, v254, v102\n\t"
    "v_mfma_f32_16x16x32_bf16 v[212:215], a[124:127], a[188:191], v[212:215]\n\t"
    "s_waitcnt lgkmcnt(4)\n\t"
    "v_add_f32 v103, v255, v103\n\t"
    "v_add_f32 v108, v252, v108\n\t"
    "v_mfma_f32_16x16x32_bf16 v[216:219], a[96:99], a[192:195], v[216:219]\n\t"
    "buffer_load_dwordx4 a[96:99], v224, s[36:39], s48 offen offset:0 sc1\n\t"
    "v_add_f32 v109, v253, v109\n\t"
    "v_add_f32 v110, v254, v110\n\t"
    "v_mfma_f32_16x16x32_bf16 v[220:223], a[100:103], a[192:195], v[220:223]\n\t"
    "v_add_f32 v111, v255, v111\n\t"
    "v_add_f32 v116, v252, v116\n\t"
    "v_mfma_f32_16x16x32_bf16 v[216:219], a[104:107], a[196:199], v[216:219]\n\t"
    "v_cvt_pk_bf16_f32 v208, v208, v209\n\t"
    "v_cvt_pk_bf16_f32 v209, v210, v211\n\t"
    "v_mfma_f32_16x16x32_bf16 v[220:223], a[108:111], a[196:199], v[220:223]\n\t"
    "s_waitcnt lgkmcnt(1)\n\t"
    "v_cvt_pk_bf16_f32 v210, v212, v213\n\t"
    "v_cvt_pk_bf16_f32 v211, v214, v215\n\t"
    "v_mfma_f32_16x16x32_bf16 v[216:219], a[112:115], a[200:203], v[216:219]\n\t"
    "ds_write_b128 v224, v[208:211] offset:2048\n\t"
    "v_mfma_f32_16x16x32_bf16 v[220:223], a[116:119], a[200:203], v[220:223]\n\t"
    "buffer_load_dwordx4 a[100:103], v224, s[36:39], s48 offen offset:1024 sc1\n\t"
    "v_add_f32 v117, v253, v117\n\t"
    "v_add_f32 v118, v254, v118\n\t"
    "v_mfma_f32_16x16x32_bf16 v[216:219], a[120:123], a[204:207], v[216:219]\n\t"
    "v_add_f32 v119, v255, v119\n\t"
    "v_add_f32 v124, v252, v124\n\t"
    "v_mfma_f32_16x16x32_bf16 v[220:223], a[124:127], a[204:207], v[220:223]\n\t"
    "s_waitcnt vmcnt(22)\n\t"
    "v_add_f32 v125, v253, v125\n\t"
    "v_add_f32 v126, v254, v126\n\t"
    "v_mfma_f32_16x16x32_bf16 v[0:3], a[0:3], v[192:195], v[0:3]\n\t"
    "buffer_load_dwordx4 a[104:107], v224, s[36:39], s48 offen offset:2048 sc1\n\t"
    "v_add_f32 v127, v255, v127\n\t"
    "v_mfma_f32_16x16x32_bf16 v[4:7], a[4:7], v[192:195], v[4:7]\n\t"
    "v_mfma_f32_16x16x32_bf16 v[32:35], a[8:11], v[192:195], v[32:35]\n\t"
    "v_cvt_pk_bf16_f32 v216, v216, v217\n\t"
    "v_cvt_pk_bf16_f32 v217, v218, v219\n\t"
    "v_mfma_f32_16x16x32_bf16 v[36:39], a[12:15], v[192:195], v[36:39]\n\t"
    "v_cvt_pk_bf16_f32 v218, v220, v221\n\t"
    "v_cvt_pk_bf16_f32 v219, v222, v223\n\t"
    "v_mfma_f32_16x16x32_bf16 v[8:11], a[0:3], v[200:203], v[8:11]\n\t"
    "ds_write_b128 v224, v[216:219] offset:3072\n\t"
    "v_mfma_f32_16x16x32_bf16 v[12:15], a[4:7], v[200:203], v[12:15]\n\t"
    "buffer_load_dwordx4 a[108:111], v224, s[36:39], s48 offen offset:3072 sc1\n\t"
    "s_add_u32 s48, s48, 4096\n\t"
    "v_mfma_f32_16x16x32_bf16 v[40:43], a[8:11], v[200:203], v[40:43]\n\t"
    "v_mfma_f32_16x16x32_bf16 v[44:47], a[12:15], v[200:203], v[44:47]\n\t"
    "s_waitcnt lgkmcnt(0)\n\t"
    "s_barrier\n\t"
    "v_mfma_f32_16x16x32_bf16 v[16:19], a[0:3], v[208:211], v[16:19]\n\t"
    "ds_read_b128 a[128:131], v227 offset:0\n\t"
    "ds_read_b128 a[144:147], v228 offset:0\n\t"
    "v_mfma_f32_16x16x32_bf16 v[20:23], a[4:7], v[208:211], v[20:23]\n\t"
    "ds_read_b128 a[160:163], v229 offset:0\n\t"
    "ds_read_b128 a[132:135], v227 offset:1024\n\t"
    "v_mfma_f32_16x16x32_bf16 v[48:51], a[8:11], v[208:211], v[48:51]\n\t"
    "ds_read_b128 a[148:151], v228 offset:1024\n\t"
    "ds_read_b128 a[164:167], v229 offset:1024\n\t"
    "v_mfma_f32_16x16x32_bf16 v[52:55], a[12:15], v[208:211], v[52:55]\n\t"
    "ds_read_b128 a[136:139], v227 offset:2048\n\t"
    "ds_read_b128 a[152:155], v228 offset:2048\n\t"
    "v_mfma_f32_16x16x32_bf16 v[24:27], a[0:3], v[216:219], v[24:27]\n\t"
    "ds_read_b128 a[168:171], v229 offset:2048\n\t"
    "ds_read_b128 a[140:143], v227 offset:3072\n\t"
    "v_mfma_f32_16x16x32_bf16 v[28:31], a[4:7], v[216:219], v[28:31]\n\t"
    "ds_read_b128 a[156:159], v228 offset:3072\n\t"
    "ds_read_b128 a[172:175], v229 offset:3072\n\t"
    "v_mfma_f32_16x16x32_bf16 v[56:59], a[8:11], v[216:219], v[56:59]\n\t"
    "buffer_load_dwordx4 a[112:115], v224, s[36:39], s48 offen offset:0 sc1\n\t"
    "buffer_load_dwordx4 a[116:119], v224, s[36:39], s48 offen offset:1024 sc1\n\t"
    "v_mfma_f32_16x16x32_bf16 v[60:63], a[12:15], v[216:219], v[60:63]\n\t"
    "s_waitcnt vmcnt(22)\n\t"
    "ds_read_b128 v[196:199], v224 offset:4096\n\t"
    "v_mfma_f32_16x16x32_bf16 v[64:67], a[16:19], v[192:195], v[64:67]\n\t"
    "buffer_load_dwordx4 a[120:123], v224, s[36:39], s48 offen offset:2048 sc1\n\t"
    "ds_read_b128 v[204:207], v224 offset:5120\n\t"
    "v_mfma_f32_16x16x32_bf16 v[68:71], a[20:23], v[192:195], v[68:71]\n\t"
    "s_waitcnt lgkmcnt(9)\n\t"
    "ds_read_b128 v[212:215], v224 offset:6144\n\t"
    "ds_read_b128 v[220:223], v224 offset:7168\n\t"
    "v_mfma_f32_16x16x32_bf16 v[72:75], a[16:19], v[200:203], v[72:75]\n\t"
    "v_mfma_f32_16x16x32_bf16 v[76:79], a[20:23], v[200:203], v[76:79]\n\t"
    "v_mfma_f32_16x16x32_bf16 v[80:83], a[16:19], v[208:211], v[80:83]\n\t"
    "buffer_load_dwordx4 a[124:127], v224, s[36:39], s48 offen offset:3072 sc1\n\t"
    "s_add_u32 s48, s48, 4096\n\t"
    "v_mfma_f32_16x16x32_bf16 v[84:87], a[20:23], v[208:211], v[84:87]\n\t"
    "s_sub_u32 s49, s49, 1\n\t"
    "s_cmp_eq_u32 s49, 0\n\t"
    "s_cbranch_scc1 Llast%=\n\t"
    "v_mfma_f32_16x16x32_bf16 v[88:91], a[16:19], v[216:219], v[88:91]\n\t"
    "v_mfma_f32_16x16x32_bf16 v[92:95], a[20:23], v[216:219], v[92:95]\n\t"
    "v_mfma_f32_16x16x32_bf16 v[0:3], a[24:27], a[128:131], v[0:3]\n\t"
    "buffer_load_dwordx4 a[0:3], v224, s[36:39], s48 offen offset:0 sc1\n\t"
    "v_mfma_f32_16x16x32_bf16 v[0:3], a[28:31], a[144:147], v[0:3]\n\t"
    "s_waitcnt vmcnt(21)\n\t"
    "v_mfma_f32_16x16x32_bf16 v[0:3], a[32:35], a[160:163], v[0:3]\n\t"
    "v_mfma_f32_16x16x32_bf16 v[4:7], a[36:39], a[128:131], v[4:7]\n\t"
    "v_mfma_f32_16x16x32_bf16 v[4:7], a[40:43], a[144:147], v[4:7]\n\t"
    "buffer_load_dwordx4 a[4:7], v224, s[36:39], s48 offen offset:1024 sc1\n\t"
    "v_mfma_f32_16x16x32_bf16 v[4:7], a[44:47], a[160:163], v[4:7]\n\t"
    "v_mfma_f32_16x16x32_bf16 v[8:11], a[24:27], a[132:135], v[8:11]\n\t"
    "v_mfma_f32_16x16x32_bf16 v[8:11], a[28:31], a[148:151], v[8:11]\n\t"
    "s_waitcnt lgkmcnt(10)\n\t"
    "v_mfma_f32_16x16x32_bf16 v[8:11], a[32:35], a[164:167], v[8:11]\n\t"
    "s_waitcnt lgkmcnt(2)\n\t"
    "buffer_load_dwordx4 a[8:11], v224, s[36:39], s48 offen offset:2048 sc1\n\t"
    "v_mul_f32 v232, v196, v0\n\t"
    "v_mfma_f32_16x16x32_bf16 v[12:15], a[36:39], a[132:135], v[12:15]\n\t"
    "v_mul_f32 v233, v197, v1\n\t"
    "v_mul_f32 v234, v198, v2\n\t"
    "v_mfma_f32_16x16x32_bf16 v[12:15], a[40:43], a[148:151], v[12:15]\n\t"
    "v_mul_f32 v235, v199, v3\n\t"
    "v_cvt_pk_bf16_f32 v232, v232, v233\n\t"
    "v_mfma_f32_16x16x32_bf16 v[12:15], a[44:47], a[164:167], v[12:15]\n\t"
    "v_mul_f32 v236, v204, v4\n\t"
    "v_mul_f32 v237, v205, v5\n\t"
    "v_mfma_f32_16x16x32_bf16 v[16:19], a[24:27], a[136:139], v[16:19]\n\t"
    "buffer_load_dwordx4 a[12:15], v224, s[36:39], s48 offen offset:3072 sc1\n\t"
    "s_add_u32 s48, s48, 4096\n\t"
    "v_cvt_pk_bf16_f32 v233, v234, v235\n\t"
    "v_mfma_f32_16x16x32_bf16 v[16:19], a[28:31], a[152:155], v[16:19]\n\t"
    "v_mul_f32 v238, v206, v6\n\t"
    "v_mul_f32 v239, v207, v7\n\t"
    "v_mfma_f32_16x16x32_bf16 v[16:19], a[32:35], a[168:171], v[16:19]\n\t"
    "ds_read_b128 v[196:199], v224 offset:8192\n\t"
    "ds_read_b128 v[204:207], v224 offset:9216\n\t"
    "v_cvt_pk_bf16_f32 v234, v236, v237\n\t"
    "v_mfma_f32_16x16x32_bf16 v[20:23], a[36:39], a[136:139], v[20:23]\n\t"
    "v_cvt_pk_bf16_f32 v235, v238, v239\n\t"
    "v_mfma_f32_16x16x32_bf16 v[20:23], a[40:43], a[152:155], v[20:23]\n\t"
    "buffer_load_dwordx4 a[16:19], v224, s[36:39], s48 offen offset:0 sc1\n\t"
    "v_mfma_f32_16x16x32_bf16 v[20:23], a[44:47], a[168:171], v[20:23]\n\t"
    "ds_write_b128 v226, v[232:235] offset:0\n\t"
    "v_mfma_f32_16x16x32_bf16 v[24:27], a[24:27], a[140:143], v[24:27]\n\t"
    "s_waitcnt lgkmcnt(3)\n\t"
    "v_mul_f32 v240, v212, v8\n\t"
    "v_mul_f32 v241, v213, v9\n\t"
    "v_mfma_f32_16x16x32_bf16 v[24:27], a[28:31], a[156:159], v[24:27]\n\t"
    "v_mul_f32 v242, v214, v10\n\t"
    "v_mul_f32 v243, v215, v11\n\t"
    "v_mfma_f32_16x16x32_bf16 v[24:27], a[32:35], a[172:175], v[24:27]\n\t"
    "buffer_load_dwordx4 a[20:23], v224, s[36:39], s48 offen offset:1024 sc1\n\t"
    "v_cvt_pk_bf16_f32 v240, v240, v241\n\t"
    "v_mul_f32 v244, v220, v12\n\t"
    "v_mfma_f32_16x16x32_bf16 v[28:31], a[36:39], a[140:143], v[28:31]\n\t"
    "v_mul_f32 v245, v221, v13\n\t"
    "v_cvt_pk_bf16_f32 v241, v242, v243\n\t"
    "v_mfma_f32_16x16x32_bf16 v[28:31], a[40:43], a[156:159], v[28:31]\n\t"
    "v_mul_f32 v246, v222, v14\n\t"
    "v_mul_f32 v247, v223, v15\n\t"
    "v_mfma_f32_16x16x32_bf16 v[28:31], a[44:47], a[172:175], v[28:31]\n\t"
    "s_waitcnt vmcnt(22)\n\t"
    "ds_read_b128 v[212:215], v224 offset:10240\n\t"
    "v_cvt_pk_bf16_f32 v242, v244, v245\n\t"
    "v_mfma_f32_16x16x32_bf16 v[32:35], a[48:51], a[128:131], v[32:35]\n\t"
    "buffer_load_dwordx4 a[24:27], v224, s[36:39], s48 offen offset:2048 sc1\n\t"
    "ds_read_b128 v[220:223], v224 offset:11264\n\t"
    "v_cvt_pk_bf16_f32 v243, v246, v247\n\t"
    "v_mfma_f32_16x16x32_bf16 v[32:35], a[52:55], a[144:147], v[32:35]\n\t"
    "ds_write_b128 v226, v[240:243] offset:8320\n\t"
    "v_mfma_f32_16x16x32_bf16 v[32:35], a[56:59], a[160:163], v[32:35]\n\t"
    "s_waitcnt lgkmcnt(4)\n\t"
    "ds_read_b128 v[248:251], v230 offset:0\n\t"
    "v_mul_f32 v232, v196, v16\n\t"
    "v_mul_f32 v233, v197, v17\n\t"
    "v_mfma_f32_16x16x32_bf16 v[36:39], a[60:63], a[128:131], v[36:39]\n\t"
    "s_waitcnt vmcnt(19)\n\t"
    "v_mul_f32 v234, v198, v18\n\t"
    "v_mul_f32 v235, v199, v19\n\t"
    "v_mfma_f32_16x16x32_bf16 v[36:39], a[64:67], a[144:147], v[36:39]\n\t"
    "buffer_load_dwordx4 a[28:31], v224, s[36:39], s48 offen offset:3072 sc1\n\t"
    "s_add_u32 s48, s48, 4096\n\t"
    "v_cvt_pk_bf16_f32 v232, v232, v233\n\t"
    "v_mfma_f32_16x16x32_bf16 v[36:39], a[68:71], a[160:163], v[36:39]\n\t"
    "ds_read_b128 v[252:255], v230 offset:16\n\t"
    "v_mul_f32 v236, v204, v20\n\t"
    "v_mul_f32 v237, v205, v21\n\t"
    "v_mfma_f32_16x16x32_bf16 v[40:43], a[48:51], a[132:135], v[40:43]\n\t"
    "v_cvt_pk_bf16_f32 v233, v234, v235\n\t"
    "v_mul_f32 v238, v206, v22\n\t"
    "v_mfma_f32_16x16x32_bf16 v[40:43], a[52:55], a[148:151], v[40:43]\n\t"
    "v_mul_f32 v239, v207, v23\n\t"
    "v_cvt_pk_bf16_f32 v234, v236, v237\n\t"
    "v_mfma_f32_16x16x32_bf16 v[40:43], a[56:59], a[164:167], v[40:43]\n\t"
    "buffer_load_dwordx4 a[32:35], v224, s[36:39], s48 offen offset:0 sc1\n\t"
    "ds_read_b128 v[196:199], v224 offset:12288\n\t"
    "v_cvt_pk_bf16_f32 v235, v238, v239\n\t"
    "v_mfma_f32_16x16x32_bf16 v[44:47], a[60:63], a[132:135], v[44:47]\n\t"
    "ds_read_b128 v[204:207], v224 offset:13312\n\t"
    "v_mfma_f32_16x16x32_bf16 v[44:47], a[64:67], a[148:151], v[44:47]\n\t"
    "ds_write_b128 v226, v[232:235] offset:16640\n\t"
    "v_mfma_f32_16x16x32_bf16 v[44:47], a[68:71], a[164:167], v[44:47]\n\t"
    "s_waitcnt lgkmcnt(6)\n\t"
    "v_mul_f32 v240, v212, v24\n\t"
    "v_mul_f32 v241, v213, v25\n\t"
    "v_mfma_f32_16x16x32_bf16 v[48:51], a[48:51], a[136:139], v[48:51]\n\t"
    "buffer_load_dwordx4 a[36:39], v224, s[36:39], s48 offen offset:1024 sc1\n\t"
    "v_mul_f32 v242, v214, v26\n\t"
    "v_mul_f32 v243, v215, v27\n\t"
    "v_mfma_f32_16x16x32_bf16 v[48:51], a[52:55], a[152:155], v[48:51]\n\t"
    "v_cvt_pk_bf16_f32 v240, v240, v241\n\t"
    "v_mul_f32 v244, v220, v28\n\t"
    "v_mfma_f32_16x16x32_bf16 v[48:51], a[56:59], a[168:171], v[48:51]\n\t"
    "v_mul_f32 v245, v221, v29\n\t"
    "v_cvt_pk_bf16_f32 v241, v242, v243\n\t"
    "v_mfma_f32_16x16x32_bf16 v[52:55], a[60:63], a[136:139], v[52:55]\n\t"
    "v_mul_f32 v246, v222, v30\n\t"
    "v_mul_f32 v247, v223, v31\n\t"
    "v_mfma_f32_16x16x32_bf16 v[52:55], a[64:67], a[152:155], v[52:55]\n\t"
    "buffer_load_dwordx4 a[40:43], v224, s[36:39], s48 offen offset:2048 sc1\n\t"
    "ds_read_b128 v[212:215], v224 offset:14336\n\t"
    "v_cvt_pk_bf16_f32 v242, v244, v245\n\t"
    "v_mfma_f32_16x16x32_bf16 v[52:55], a[68:71], a[168:171], v[52:55]\n\t"
    "s_waitcnt lgkmcnt(5)\n\t"
    "ds_read_b128 v[220:223], v224 offset:15360\n\t"
    "v_cvt_pk_bf16_f32 v243, v246, v247\n\t"
    "v_add_f32 v0, v248, v0\n\t"
    "v_mfma_f32_16x16x32_bf16 v[56:59], a[48:51], a[140:143], v[56:59]\n\t"
    "ds_write_b128 v226, v[240:243] offset:24960\n\t"
    "v_mfma_f32_16x16x32_bf16 v[56:59], a[52:55], a[156:159], v[56:59]\n\t"
    "s_waitcnt lgkmcnt(4)\n\t"
    "v_mul_f32 v232, v196, v32\n\t"
    "v_mul_f32 v233, v197, v33\n\t"
    "v_mfma_f32_16x16x32_bf16 v[56:59], a[56:59], a[172:175], v[56:59]\n\t"
    "buffer_load_dwordx4 a[44:47], v224, s[36:39], s48 offen offset:3072 sc1\n\t"
    "s_add_u32 s48, s48, 4096\n\t"
    "v_mul_f32 v234, v198, v34\n\t"
    "v_mfma_f32_16x16x32_bf16 v[60:63], a[60:63], a[140:143], v[60:63]\n\t"
    "v_mul_f32 v235, v199, v35\n\t"
    "v_cvt_pk_bf16_f32 v232, v232, v233\n\t"
    "v_mfma_f32_16x16x32_bf16 v[60:63], a[64:67], a[156:159], v[60:63]\n\t"
    "v_mul_f32 v236, v204, v36\n\t"
    "v_mul_f32 v237, v205, v37\n\t"
    "v_mfma_f32_16x16x32_bf16 v[60:63], a[68:71], a[172:175], v[60:63]\n\t"
    "v_cvt_pk_bf16_f32 v233, v234, v235\n\t"
    "v_mul_f32 v238, v206, v38\n\t"
    "v_mfma_f32_16x16x32_bf16 v[64:67], a[72:75], a[128:131], v[64:67]\n\t"
    "buffer_load_dwordx4 a[48:51], v224, s[36:39], s48 offen offset:0 sc1\n\t"
    "v_mul_f32 v239, v207, v39\n\t"
    "v_cvt_pk_bf16_f32 v234, v236, v237\n\t"
    "v_mfma_f32_16x16x32_bf16 v[64:67], a[76:79], a[144:147], v[64:67]\n\t"
    "s_waitcnt vmcnt(21)\n\t"
    "ds_read_b128 v[196:199], v224 offset:16384\n\t"
    "v_cvt_pk_bf16_f32 v235, v238, v239\n\t"
    "v_mfma_f32_16x16x32_bf16 v[64:67], a[80:83], a[160:163], v[64:67]\n\t"
    "ds_read_b128 v[204:207], v224 offset:17408\n\t"
    "v_add_f32 v1, v249, v1\n\t"
    "v_add_f32 v2, v250, v2\n\t"
    "v_mfma_f32_16x16x32_bf16 v[68:71], a[84:87], a[128:131], v[68:71]\n\t"
    "ds_write_b128 v226, v[232:235] offset:64\n\t"
    "v_mfma_f32_16x16x32_bf16 v[68:71], a[88:91], a[144:147], v[68:71]\n\t"
    "s_waitcnt lgkmcnt(4)\n\t"
    "buffer_load_dwordx4 a[52:55], v224, s[36:39], s48 offen offset:1024 sc1\n\t"
    "v_mul_f32 v240, v212, v40\n\t"
    "v_mul_f32 v241, v213, v41\n\t"
    "v_mfma_f32_16x16x32_bf16 v[68:71], a[92:95], a[160:163], v[68:71]\n\t"
    "v_mul_f32 v242, v214, v42\n\t"
    "v_mul_f32 v243, v215, v43\n\t"
    "v_mfma_f32_16x16x32_bf16 v[72:75], a[72:75], a[132:135], v[72:75]\n\t"
    "v_cvt_pk_bf16_f32 v240, v240, v241\n\t"
    "v_mul_f32 v244, v220, v44\n\t"
    "v_mfma_f32_16x16x32_bf16 v[72:75], a[76:79], a[148:151], v[72:75]\n\t"
    "v_mul_f32 v245, v221, v45\n\t"
    "v_cvt_pk_bf16_f32 v241, v242, v243\n\t"
    "v_mfma_f32_16x16x32_bf16 v[72:75], a[80:83], a[164:167], v[72:75]\n\t"
    "buffer_load_dwordx4 a[56:59], v224, s[36:39], s48 offen offset:2048 sc1\n\t"
    "v_mul_f32 v246, v222, v46\n\t"
    "v_mul_f32 v247, v223, v47\n\t"
    "v_mfma_f32_16x16x32_bf16 v[76:79], a[84:87], a[132:135], v[76:79]\n\t"
    "ds_read_b128 v[212:215], v224 offset:18432\n\t"
    "ds_read_b128 v[220:223], v224 offset:19456\n\t"
    "v_cvt_pk_bf16_f32 v242, v244, v245\n\t"
    "v_mfma_f32_16x16x32_bf16 v[76:79], a[88:91], a[148:151], v[76:79]\n\t"
    "v_cvt_pk_bf16_f32 v243, v246, v247\n\t"
    "v_add_f32 v3, v251, v3\n\t"
    "v_mfma_f32_16x16x32_bf16 v[76:79], a[92:95], a[164:167], v[76:79]\n\t"
    "ds_write_b128 v226, v[240:243] offset:8384\n\t"
    "v_mfma_f32_16x16x32_bf16 v[80:83], a[72:75], a[136:139], v[80:83]\n\t"
    "s_waitcnt lgkmcnt(4)\n\t"
    "buffer_load_dwordx4 a[60:63], v224, s[36:39], s48 offen offset:3072 sc1\n\t"
    "s_add_u32 s48, s48, 4096\n\t"
    "v_mul_f32 v232, v196, v48\n\t"
    "v_mfma_f32_16x16x32_bf16 v[80:83], a[76:79], a[152:155], v[80:83]\n\t"
    "v_mul_f32 v233, v197, v49\n\t"
    "v_mul_f32 v234, v198, v50\n\t"
    "v_mfma_f32_16x16x32_bf16 v[80:83], a[80:83], a[168:171], v[80:83]\n\t"
    "v_mul_f32 v235, v199, v51\n\t"
    "v_cvt_pk_bf16_f32 v232, v232, v233\n\t"
    "v_mfma_f32_16x16x32_bf16 v[84:87], a[84:87], a[136:139], v[84:87]\n\t"
    "v_mul_f32 v236, v204, v52\n\t"
    "v_mul_f32 v237, v205, v53\n\t"
    "v_mfma_f32_16x16x32_bf16 v[84:87], a[88:91], a[152:155], v[84:87]\n\t"
    "buffer_load_dwordx4 a[64:67], v224, s[36:39], s48 offen offset:0 sc1\n\t"
    "v_cvt_pk_bf16_f32 v233, v234, v235\n\t"
    "v_mul_f32 v238, v206, v54\n\t"
    "v_mfma_f32_16x16x32_bf16 v[84:87], a[92:95], a[168:171], v[84:87]\n\t"
    "v_mul_f32 v239, v207, v55\n\t"
    "v_cvt_pk_bf16_f32 v234, v236, v237\n\t"
    "v_mfma_f32_16x16x32_bf16 v[88:91], a[72:75], a[140:143], v[88:91]\n\t"
    "v_cvt_pk_bf16_f32 v235, v238, v239\n\t"
    "v_add_f32 v8, v248, v8\n\t"
    "v_mfma_f32_16x16x32_bf16 v[88:91], a[76:79], a[156:159], v[88:91]\n\t"
    "ds_write_b128 v226, v[232:235] offset:16704\n\t"
    "v_mfma_f32_16x16x32_bf16 v[88:91], a[80:83], a[172:175], v[88:91]\n\t"
    "s_waitcnt lgkmcnt(2)\n\t"
    "buffer_load_dwordx4 a[68:71], v224, s[36:39], s48 offen offset:1024 sc1\n\t"
    "v_mul_f32 v240, v212, v56\n\t"
    "v_mul_f32 v241, v213, v57\n\t"
    "v_mfma_f32_16x16x32_bf16 v[92:95], a[84:87], a[140:143], v[92:95]\n\t"
    "v_mul_f32 v242, v214, v58\n\t"
    "v_mul_f32 v243, v215, v59\n\t"
    "v_mfma_f32_16x16x32_bf16 v[92:95], a[88:91], a[156:159], v[92:95]\n\t"
    "v_cvt_pk_bf16_f32 v240, v240, v241\n\t"
    "v_mul_f32 v244, v220, v60\n\t"
    "v_mfma_f32_16x16x32_bf16 v[92:95], a[92:95], a[172:175], v[92:95]\n\t"
    "s_waitcnt vmcnt(22)\n\t"
    "v_mul_f32 v245, v221, v61\n\t"
    "v_cvt_pk_bf16_f32 v241, v242, v243\n\t"
    "v_mfma_f32_16x16x32_bf16 v[96:99], a[96:99], v[192:195], v[96:99]\n\t"
    "buffer_load_dwordx4 a[72:75], v224, s[36:39], s48 offen offset:2048 sc1\n\t"
    "v_mul_f32 v246, v222, v62\n\t"
    "v_mul_f32 v247, v223, v63\n\t"
    "v_mfma_f32_16x16x32_bf16 v[96:99], a[100:103], a[128:131], v[96:99]\n\t"
    "v_cvt_pk_bf16_f32 v242, v244, v245\n\t"
    "v_cvt_pk_bf16_f32 v243, v246, v247\n\t"
    "v_mfma_f32_16x16x32_bf16 v[96:99], a[104:107], a[144:147], v[96:99]\n\t"
    "ds_write_b128 v226, v[240:243] offset:25024\n\t"
    "v_mfma_f32_16x16x32_bf16 v[96:99], a[108:111], a[160:163], v[96:99]\n\t"
    "s_waitcnt vmcnt(19)\n\t"
    "v_mul_f32 v232, v128, v64\n\t"
    "v_mul_f32 v233, v129, v65\n\t"
    "v_mfma_f32_16x16x32_bf16 v[100:103], a[112:115], v[192:195], v[100:103]\n\t"
    "buffer_load_dwordx4 a[76:79], v224, s[36:39], s48 offen offset:3072 sc1\n\t"
    "s_add_u32 s48, s48, 4096\n\t"
    "v_mul_f32 v234, v130, v66\n\t"
    "v_mfma_f32_16x16x32_bf16 v[100:103], a[116:119], a[128:131], v[100:103]\n\t"
    "v_mul_f32 v235, v131, v67\n\t"
    "v_cvt_pk_bf16_f32 v232, v232, v233\n\t"
    "v_mfma_f32_16x16x32_bf16 v[100:103], a[120:123], a[144:147], v[100:103]\n\t"
    "v_mul_f32 v236, v132, v68\n\t"
    "v_mul_f32 v237, v133, v69\n\t"
    "v_mfma_f32_16x16x32_bf16 v[100:103], a[124:127], a[160:163], v[100:103]\n\t"
    "v_cvt_pk_bf16_f32 v233, v234, v235\n\t"
    "v_mul_f32 v238, v134, v70\n\t"
    "v_mfma_f32_16x16x32_bf16 v[104:107], a[96:99], v[200:203], v[104:107]\n\t"
    "buffer_load_dwordx4 a[80:83], v224, s[36:39], s48 offen offset:0 sc1\n\t"
    "v_mul_f32 v239, v135, v71\n\t"
    "v_cvt_pk_bf16_f32 v234, v236, v237\n\t"
    "v_mfma_f32_16x16x32_bf16 v[104:107], a[100:103], a[132:135], v[104:107]\n\t"
    "v_cvt_pk_bf16_f32 v235, v238, v239\n\t"
    "v_add_f32 v9, v249, v9\n\t"
    "v_mfma_f32_16x16x32_bf16 v[104:107], a[104:107], a[148:151], v[104:107]\n\t"
    "ds_write_b128 v226, v[232:235] offset:33280\n\t"
    "v_mfma_f32_16x16x32_bf16 v[104:107], a[108:111], a[164:167], v[104:107]\n\t"
    "v_mul_f32 v240, v136, v72\n\t"
    "v_mul_f32 v241, v137, v73\n\t"
    "v_mfma_f32_16x16x32_bf16 v[108:111], a[112:115], v[200:203], v[108:111]\n\t"
    "buffer_load_dwordx4 a[84:87], v224, s[36:39], s48 offen offset:1024 sc1\n\t"
    "v_mul_f32 v242, v138, v74\n\t"
    "v_mul_f32 v243, v139, v75\n\t"
    "v_mfma_f32_16x16x32_bf16 v[108:111], a[116:119], a[132:135], v[108:111]\n\t"
    "v_cvt_pk_bf16_f32 v240, v240, v241\n\t"
    "v_mul_f32 v244, v140, v76\n\t"
    "v_mfma_f32_16x16x32_bf16 v[108:111], a[120:123], a[148:151], v[108:111]\n\t"
    "v_mul_f32 v245, v141, v77\n\t"
    "v_cvt_pk_bf16_f32 v241, v242, v243\n\t"
    "v_mfma_f32_16x16x32_bf16 v[108:111], a[124:127], a[164:167], v[108:111]\n\t"
    "v_mul_f32 v246, v142, v78\n\t"
    "v_mul_f32 v247, v143, v79\n\t"
    "v_mfma_f32_16x16x32_bf16 v[112:115], a[96:99], v[208:211], v[112:115]\n\t"
    "buffer_load_dwordx4 a[88:91], v224, s[36:39], s48 offen offset:2048 sc1\n\t"
    "v_cvt_pk_bf16_f32 v242, v244, v245\n\t"
    "v_cvt_pk_bf16_f32 v243, v246, v247\n\t"
    "v_mfma_f32_16x16x32_bf16 v[112:115], a[100:103], a[136:139], v[112:115]\n\t"
    "ds_write_b128 v226, v[240:243] offset:41600\n\t"
    "v_mfma_f32_16x16x32_bf16 v[112:115], a[104:107], a[152:155], v[112:115]\n\t"
    "v_mul_f32 v232, v144, v80\n\t"
    "v_mul_f32 v233, v145, v81\n\t"
    "v_mfma_f32_16x16x32_bf16 v[112:115], a[108:111], a[168:171], v[112:115]\n\t"
    "s_waitcnt lgkmcnt(0)\n\t"
    "s_barrier\n\t"
    "v_mfma_f32_16x16x32_bf16 v[116:119], a[112:115], v[208:211], v[116:119]\n\t"
    "ds_read_b128 a[176:179], v225 offset:0\n\t"
    "buffer_load_dwordx4 a[92:95], v224, s[36:39], s48 offen offset:3072 sc1\n\t"
    "s_add_u32 s48, s48, 4096\n\t"
    "v_mfma_f32_16x16x32_bf16 v[116:119], a[116:119], a[136:139], v[116:119]\n\t"
    "ds_read_b128 a[180:183], v225 offset:8320\n\t"
    "v_mul_f32 v234, v146, v82\n\t"
    "v_mul_f32 v235, v147, v83\n\t"
    "v_mfma_f32_16x16x32_bf16 v[116:119], a[120:123], a[152:155], v[116:119]\n\t"
    "v_cvt_pk_bf16_f32 v232, v232, v233\n\t"
    "v_mul_f32 v236, v148, v84\n\t"
    "v_mfma_f32_16x16x32_bf16 v[116:119], a[124:127], a[168:171], v[116:119]\n\t"
    "v_mul_f32 v237, v149, v85\n\t"
    "v_cvt_pk_bf16_f32 v233, v234, v235\n\t"
    "v_mfma_f32_16x16x32_bf16 v[120:123], a[96:99], v[216:219], v[120:123]\n\t"
    "ds_read_b128 a[184:187], v225 offset:16640\n\t"
    "buffer_load_dwordx4 a[96:99], v224, s[36:39], s48 offen offset:0 sc1\n\t"
    "v_mul_f32 v238, v150, v86\n\t"
    "v_mfma_f32_16x16x32_bf16 v[120:123], a[100:103], a[140:143], v[120:123]\n\t"
    "ds_read_b128 a[188:191], v225 offset:24960\n\t"
    "v_mul_f32 v239, v151, v87\n\t"
    "v_cvt_pk_bf16_f32 v234, v236, v237\n\t"
    "v_mfma_f32_16x16x32_bf16 v[120:123], a[104:107], a[156:159], v[120:123]\n\t"
    "v_cvt_pk_bf16_f32 v235, v238, v239\n\t"
    "v_add_f32 v10, v250, v10\n\t"
    "v_mfma_f32_16x16x32_bf16 v[120:123], a[108:111], a[172:175], v[120:123]\n\t"
    "ds_write_b128 v226, v[232:235] offset:49920\n\t"
    "v_mfma_f32_16x16x32_bf16 v[124:127], a[112:115], v[216:219], v[124:127]\n\t"
    "ds_read_b128 a[192:195], v225 offset:16\n\t"
    "buffer_load_dwordx4 a[100:103], v224, s[36:39], s48 offen offset:1024 sc1\n\t"
    "v_mul_f32 v240, v152, v88\n\t"
    "v_mfma_f32_16x16x32_bf16 v[124:127], a[116:119], a[140:143], v[124:127]\n\t"
    "ds_read_b128 a[196:199], v225 offset:8336\n\t"
    "v_mul_f32 v241, v153, v89\n\t"
    "v_mul_f32 v242, v154, v90\n\t"
    "v_mfma_f32_16x16x32_bf16 v[124:127], a[120:123], a[156:159], v[124:127]\n\t"
    "v_mul_f32 v243, v155, v91\n\t"
    "v_cvt_pk_bf16_f32 v240, v240, v241\n\t"
    "v_mfma_f32_16x16x32_bf16 v[124:127], a[124:127], a[172:175], v[124:127]\n\t"
    "s_branch Lloop%=\n\t"
    "Lend%=:\n\t"
    :
    : "s"(ka), "s"(w), "s"(ldsb), "s"(blk)
    : "v0", "v1", "v2", "v3", "v4", "v5", "v6", "v7", "v8", "v9", "v10", "v11", "v12", "v13", "v14", "v15", "v16", "v17", "v18", "v19", "v20", "v21", "v22", "v23", "v24", "v25", "v26", "v27", "v28", "v29", "v30", "v31", "v32", "v33", "v34", "v35", "v36", "v37", "v38", "v39", "v40", "v41", "v42", "v43", "v44", "v45", "v46", "v47", "v48", "v49", "v50", "v51", "v52", "v53", "v54", "v55", "v56", "v57", "v58", "v59", "v60", "v61", "v62", "v63", "v64", "v65", "v66", "v67", "v68", "v69", "v70", "v71", "v72", "v73", "v74", "v75", "v76", "v77", "v78", "v79", "v80", "v81", "v82", "v83", "v84", "v85", "v86", "v87", "v88", "v89", "v90", "v91", "v92", "v93", "v94", "v95", "v96", "v97", "v98", "v99", "v100", "v101", "v102", "v103", "v104", "v105", "v106", "v107", "v108", "v109", "v110", "v111", "v112", "v113", "v114", "v115", "v116", "v117", "v118", "v119", "v120", "v121", "v122", "v123", "v124", "v125", "v126", "v127", "v128", "v129", "v130", "v131", "v132", "v133", "v134", "v135", "v136", "v137", "v138", "v139", "v140", "v141", "v142", "v143", "v144", "v145", "v146", "v147", "v148", "v149", "v150", "v151", "v152", "v153", "v154", "v155", "v156", "v157", "v158", "v159", "v160", "v161", "v162", "v163", "v164", "v165", "v166", "v167", "v168", "v169", "v170", "v171", "v172", "v173", "v174", "v175", "v176", "v177", "v178", "v179", "v180", "v181", "v182", "v183", "v184", "v185", "v186", "v187", "v188", "v189", "v190", "v191", "v192", "v193", "v194", "v195", "v196", "v197", "v198", "v199", "v200", "v201", "v202", "v203", "v204", "v205", "v206", "v207", "v208", "v209", "v210", "v211", "v212", "v213", "v214", "v215", "v216", "v217", "v218", "v219", "v220", "v221", "v222", "v223", "v224", "v225", "v226", "v227", "v228", "v229", "v230", "v231", "v232", "v233", "v234", "v235", "v236", "v237", "v238", "v239", "v240", "v241", "v242", "v243", "v244", "v245", "v246", "v247", "v248", "v249", "v250", "v251", "v252", "v253", "v254", "v255", "a0", "a1", "a2", "a3", "a4", "a5", "a6", "a7", "a8", "a9", "a10", "a11", "a12", "a13", "a14", "a15", "a16", "a17", "a18", "a19", "a20", "a21", "a22", "a23", "a24", "a25", "a26", "a27", "a28", "a29", "a30", "a31", "a32", "a33", "a34", "a35", "a36", "a37", "a38", "a39", "a40", "a41", "a42", "a43", "a44", "a45", "a46", "a47", "a48", "a49", "a50", "a51", "a52", "a53", "a54", "a55", "a56", "a57", "a58", "a59", "a60", "a61", "a62", "a63", "a64", "a65", "a66", "a67", "a68", "a69", "a70", "a71", "a72", "a73", "a74", "a75", "a76", "a77", "a78", "a79", "a80", "a81", "a82", "a83", "a84", "a85", "a86", "a87", "a88", "a89", "a90", "a91", "a92", "a93", "a94", "a95", "a96", "a97", "a98", "a99", "a100", "a101", "a102", "a103", "a104", "a105", "a106", "a107", "a108", "a109", "a110", "a111", "a112", "a113", "a114", "a115", "a116", "a117", "a118", "a119", "a120", "a121", "a122", "a123", "a124", "a125", "a126", "a127", "a128", "a129", "a130", "a131", "a132", "a133", "a134", "a135", "a136", "a137", "a138", "a139", "a140", "a141", "a142", "a143", "a144", "a145", "a146", "a147", "a148", "a149", "a150", "a151", "a152", "a153", "a154", "a155", "a156", "a157", "a158", "a159", "a160", "a161", "a162", "a163", "a164", "a165", "a166", "a167", "a168", "a169", "a170", "a171", "a172", "a173", "a174", "a175", "a176", "a177", "a178", "a179", "a180", "a181", "a182", "a183", "a184", "a185", "a186", "a187", "a188", "a189", "a190", "a191", "a192", "a193", "a194", "a195", "a196", "a197", "a198", "a199", "a200", "a201", "a202", "a203", "a204", "a205", "a206", "a207", "a208", "a209", "a210", "a211", "a212", "a213", "a214", "a215", "a216", "a217", "a218", "a219", "a220", "a221", "a222", "a223", "a224", "a225", "a226", "a227", "a228", "a229", "a230", "a231", "a232", "a233", "a234", "a235", "a236", "a237", "a238", "a239", "a240", "a241", "a242", "a243", "a244", "a245", "a246", "a247", "a248", "a249", "a250", "a251", "a252", "a253", "a254", "a255", "s12", "s13", "s14", "s15", "s16", "s17", "s18", "s19", "s20", "s21", "s22", "s23", "s24", "s25", "s26", "s27", "s28", "s29", "s30", "s31", "s36", "s37", "s38", "s39", "s40", "s41", "s42", "s43", "s44", "s45", "s46", "s47", "s48", "s49", "s50", "s51", "s52", "s53", "s54", "s55", "s56", "s57", "s58", "s59", "s60", "s61", "s62", "s63", "s64", "s65", "s66", "s67", "s68", "s69", "s70", "s71", "s72", "s73", "s74", "s75", "s76", "s77", "s78", "s79", "s80", "s81", "s82", "s83", "s84", "s85", "s86", "s87", "s88", "s89", "s90", "s91", "s92", "s93", "s94", "s95", "s96", "s97", "s98", "s99", "vcc", "scc", "memory");
}

extern "C" void kernel_launch(void* const* d_in, const int* in_sizes, int n_in, void* d_out, int out_size, void* d_ws, size_t ws_size,
                              hipStream_t stream) {
  dcn_main<<<NB / 64, 256, 0, stream>>>((const char*)d_in[0], (const char*)d_in[1], (const char*)d_in[2], (const char*)d_in[3],
                                        (char*)d_ws, (char*)d_out);
}
```
